```python
import torch

def _softmax(x, axis=-1):
    return torch.softmax(x, dim=axis)
def _gelu(x):
    return torch.nn.functional.gelu(x, approximate="tanh")
def _silu(x):
    return torch.nn.functional.silu(x)
def _sigmoid(x):
    return torch.sigmoid(x)
def _layernorm(x, axis=-1, eps=1e-5):
    m = x.mean(dim=axis, keepdim=True)
    v = x.var(dim=axis, keepdim=True, unbiased=False)
    return (x - m) / torch.sqrt(v + eps)
def _scatter_add(tgt, idx, src, axis=0):
    return tgt.clone().index_add_(axis, idx, src)
def _segment_sum(src, seg, n):
    out = torch.zeros((n,) + src.shape[1:], dtype=src.dtype)
    return out.index_add_(0, seg, src)
def _pad(x, pw):
    flat = tuple(v for pair in reversed(pw) for v in pair)
    return torch.nn.functional.pad(x, flat)

kernel_name = 'recurrent_scan__grouped_kv_mqa__geglu_ffn__prompt'

def setup_inputs():

    _w = (lambda a: (lambda t: t.to(torch.bfloat16) if t.is_floating_point() else t)(torch.as_tensor(a)))
    def _A_setup():
        B, S, D, H = 1, 2048, 512, 32
        Dh = D // H
        return {
            "x": _w((lambda *s: torch.randn(*s, dtype=torch.bfloat16) * 0.05)(B, S, D)),
            "wq": _w((lambda *s: torch.randn(*s, dtype=torch.bfloat16) * 0.05)(D, H * Dh)),
            "wk": _w((lambda *s: torch.randn(*s, dtype=torch.bfloat16) * 0.05)(D, Dh)),
            "wv": _w((lambda *s: torch.randn(*s, dtype=torch.bfloat16) * 0.05)(D, Dh)),
            "wo": _w((lambda *s: torch.randn(*s, dtype=torch.bfloat16) * 0.05)(H * Dh, D)),
        }
    _A_in = _A_setup()

    def _B_setup():
        B, S, D, F = 1, 2048, 512, 1792
        return {
            "x": _w((lambda *s: torch.randn(*s, dtype=torch.bfloat16) * 0.05)(B, S, D)),
            "w_gate": _w((lambda *s: torch.randn(*s, dtype=torch.bfloat16) * 0.05)(D, F)),
            "w_up": _w((lambda *s: torch.randn(*s, dtype=torch.bfloat16) * 0.05)(D, F)),
            "w_down": _w((lambda *s: torch.randn(*s, dtype=torch.bfloat16) * 0.05)(F, D)),
        }
    _B_in = _B_setup()

    return {
        "A_x": _A_in["x"],
        "A_wq": _A_in["wq"],
        "A_wk": _A_in["wk"],
        "A_wv": _A_in["wv"],
        "A_wo": _A_in["wo"],
        "B_x": _B_in["x"],
        "B_w_gate": _B_in["w_gate"],
        "B_w_up": _B_in["w_up"],
        "B_w_down": _B_in["w_down"],
    }

def reference(A_x, A_wq, A_wk, A_wv, A_wo, B_x, B_w_gate, B_w_up, B_w_down):
    def _A_ref(x, wq, wk, wv, wo):
        B, S, D = x.shape
        H = 32
        Dh = D // H
        q = (lambda x, *d: x.permute(*d))((x @ wq).reshape(B, S, H, Dh), 0, 2, 1, 3)
        k = x @ wk
        v = x @ wv
        scores = torch.einsum("bhqd,bkd->bhqk", q, k) * (Dh ** -0.5)
        p = _softmax(scores, axis=-1)
        o = torch.einsum("bhqk,bkd->bhqd", p, v)
        return (lambda x, *d: x.permute(*d))(o, 0, 2, 1, 3).reshape(B, S, H * Dh) @ wo

    def _B_ref(x, w_gate, w_up, w_down):
        return (_gelu(x @ w_gate) * (x @ w_up)) @ w_down

    x = A_x
    xn = _layernorm(x, axis=-1)
    s = _A_ref(xn, A_wq, A_wk, A_wv, A_wo)
    g = _silu(_B_ref(xn, B_w_gate, B_w_up, B_w_down))
    return x + s * g
```

```cpp
#include <hip/hip_runtime.h>
#include <hip/hip_bf16.h>
#include <type_traits>

typedef __bf16 bf16x8 __attribute__((ext_vector_type(8)));
typedef __bf16 bf16x2 __attribute__((ext_vector_type(2)));
typedef short s16x8 __attribute__((ext_vector_type(8)));
typedef short v4i16_t __attribute__((ext_vector_type(4)));
typedef float f32x16 __attribute__((ext_vector_type(16)));
typedef float f32x4 __attribute__((ext_vector_type(4)));
typedef float f32x2 __attribute__((ext_vector_type(2)));
typedef unsigned u32x4 __attribute__((ext_vector_type(4)));
typedef unsigned u32x2 __attribute__((ext_vector_type(2)));
typedef unsigned short u16;
typedef unsigned long long u64;
#define DI __device__ __forceinline__
#define LAS __attribute__((address_space(3)))
#define GAS __attribute__((address_space(1)))

constexpr int SEQ = 2048, DM = 512, FF = 1792;
constexpr size_t WS_FLAG = 0;
constexpr size_t WS_KV = 32768;
constexpr size_t WS_OF = WS_KV + 72 * 3072;
constexpr size_t WS_HF = WS_OF + 4194304;
constexpr size_t WS_END = WS_HF + 64 * 112 * 1024;
constexpr int FL_KV = 0, KV_REP = 16, FL_H = 1024, FL_O = 1536, FL_TMO = 2048, FL_EPOCH = 2064;
constexpr int L_X = 0;
constexpr int L_W = 65536;
constexpr int L_U = 131072;
constexpr int L_Q = 131072;
constexpr int L_G = 139264;
constexpr int L_OX = 147456;
constexpr int L_VT = 159744;
constexpr int L_F = 160768;
constexpr int F_QP = 0, F_RD = 4, F_QF = 8, F_P1 = 12, F_DP = 16, F_KV = 21, F_FAST = 22, F_KN = 23;
constexpr int LDS_BYTES = 163840;
__device__ __forceinline__ unsigned opart(int rec) { return rec < 28 ? L_X + rec * 2304 : L_OX + (rec - 28) * 2304; }

constexpr float QSCALE = 0.25f * 1.4426950408889634f;
constexpr float FAST_BOUND = 60.0f;

DI unsigned cvtpk(float lo, float hi) { f32x2 v = {lo, hi}; bf16x2 b = __builtin_convertvector(v, bf16x2); return __builtin_bit_cast(unsigned, b); }
DI float bflo(unsigned u) { return __uint_as_float(u << 16); }
DI float bfhi(unsigned u) { return __uint_as_float(u & 0xffff0000u); }
DI u32x4 pack8u(const float* v) { return u32x4{cvtpk(v[0], v[1]), cvtpk(v[2], v[3]), cvtpk(v[4], v[5]), cvtpk(v[6], v[7])}; }
DI bf16x8 pack8(const float* v) { return __builtin_bit_cast(bf16x8, pack8u(v)); }
DI f32x16 mfma(bf16x8 a, bf16x8 b, f32x16 c) { return __builtin_amdgcn_mfma_f32_32x32x16_bf16(a, b, c, 0, 0, 0); }
DI void glds(unsigned voff, const void* sbase, unsigned lds) {
  asm volatile("s_mov_b32 m0, %2\n\ts_nop 0\n\tglobal_load_lds_dwordx4 %0, %1" :: "v"(voff), "s"(sbase), "s"(lds) : "memory");
}
template <typename T> DI T ldsr(unsigned a) { return *(const LAS T*)(size_t)a; }
template <typename T> DI void ldsw(unsigned a, T v) { *(LAS T*)(size_t)a = v; }
DI bf16x8 trfrag(unsigned p0, unsigned p1) {
  v4i16_t a = __builtin_amdgcn_ds_read_tr16_b64_v4i16((LAS v4i16_t*)(size_t)p0), b = __builtin_amdgcn_ds_read_tr16_b64_v4i16((LAS v4i16_t*)(size_t)p1);
  s16x8 r = {a[0], a[1], a[2], a[3], b[0], b[1], b[2], b[3]};
  return __builtin_bit_cast(bf16x8, r);
}
DI float dot2(unsigned a, unsigned b, float c) { return __builtin_amdgcn_fdot2_f32_bf16(__builtin_bit_cast(bf16x2, a), __builtin_bit_cast(bf16x2, b), c, false); }
DI float dot8(bf16x8 a, bf16x8 b, float c) { u32x4 x = __builtin_bit_cast(u32x4, a), y = __builtin_bit_cast(u32x4, b); c = dot2(x[0], y[0], c); c = dot2(x[1], y[1], c); c = dot2(x[2], y[2], c); return dot2(x[3], y[3], c); }
DI float sum8(bf16x8 a, float c) { u32x4 x = __builtin_bit_cast(u32x4, a); c = dot2(x[0], 0x3f803f80u, c); c = dot2(x[1], 0x3f803f80u, c); c = dot2(x[2], 0x3f803f80u, c); return dot2(x[3], 0x3f803f80u, c); }
DI u64 uni64(u64 v) { return (u64)(unsigned)__builtin_amdgcn_readfirstlane((unsigned)v) | ((u64)(unsigned)__builtin_amdgcn_readfirstlane((unsigned)(v >> 32)) << 32); }
DI float xhalf(float v) {
  const unsigned u = __float_as_uint(v);
  const auto r = __builtin_amdgcn_permlane32_swap(u, u, false, false);
  return __uint_as_float((threadIdx.x & 32) ? r[0] : r[1]);
}
template <int CTRL> DI float dpp(float v) { return __uint_as_float(__builtin_amdgcn_update_dpp(0u, __float_as_uint(v), CTRL, 0xf, 0xf, true)); }
DI float sum8lanes(float v) { v += dpp<0xB1>(v); v += dpp<0x4E>(v); return v + dpp<0x141>(v); }
DI float max64(float v) {
  v = fmaxf(v, dpp<0x121>(v)); v = fmaxf(v, dpp<0x122>(v)); v = fmaxf(v, dpp<0x124>(v)); v = fmaxf(v, dpp<0x128>(v));
  const float a = __uint_as_float(__builtin_amdgcn_readlane(__float_as_uint(v), 0)), b = __uint_as_float(__builtin_amdgcn_readlane(__float_as_uint(v), 16));
  const float c = __uint_as_float(__builtin_amdgcn_readlane(__float_as_uint(v), 32)), d = __uint_as_float(__builtin_amdgcn_readlane(__float_as_uint(v), 48));
  return fmaxf(fmaxf(a, b), fmaxf(c, d));
}
#define WAITV(n) asm volatile("s_waitcnt vmcnt(" #n ")" ::: "memory")
#define WAITL() asm volatile("s_waitcnt lgkmcnt(0)" ::: "memory")
#define BAR() do { asm volatile("s_waitcnt lgkmcnt(0)" ::: "memory"); __builtin_amdgcn_s_barrier(); asm volatile("" ::: "memory"); } while (0)

DI void st16_wt(__amdgpu_buffer_rsrc_t rs, unsigned off, u32x4 v) { __builtin_amdgcn_raw_buffer_store_b128(v, rs, off, 0, 16); }
DI u32x4 ld16_wt(__amdgpu_buffer_rsrc_t rs, unsigned off) { return __builtin_amdgcn_raw_buffer_load_b128(rs, off, 0, 16); }
DI u64 ld_word(u64* f, int i) { return __hip_atomic_load((GAS u64*)f + i, __ATOMIC_RELAXED, __HIP_MEMORY_SCOPE_AGENT); }
DI void st_word(u64* f, int i, u64 v) { __hip_atomic_store((GAS u64*)f + i, v, __ATOMIC_RELAXED, __HIP_MEMORY_SCOPE_AGENT); }
template <int SLEEP = 1> DI u64 wait_words(u64* f, int idx, bool active, u64 tag) {
  u64 v;
  for (unsigned spins = 0;; ++spins) {
    v = active ? ld_word(f, idx) : (tag << 16);
    if (__all((v >> 16) == tag)) break;
    __builtin_amdgcn_s_sleep(SLEEP);
    if ((spins & 63u) == 63u) {
      if (__any((ld_word(f, FL_TMO) >> 16) == tag)) break;
      if (spins > (1u << 16)) { st_word(f, FL_TMO, tag << 16); break; }
    }
  }
  __builtin_amdgcn_fence(__ATOMIC_ACQUIRE, "wavefront");
  asm volatile("" ::: "memory");
  return v;
}

DI unsigned geglu2(f32x2 g, f32x2 u) {
  constexpr float c1 = -2.0f * 0.7978845608028654f * 1.4426950408889634f, c3 = c1 * 0.044715f;
  const f32x2 z = g * __builtin_elementwise_fma(g * g, f32x2{c3, c3}, f32x2{c1, c1});
  const f32x2 d = f32x2{__builtin_amdgcn_exp2f(z[0]), __builtin_amdgcn_exp2f(z[1])} + f32x2{1.0f, 1.0f};
  const f32x2 y = (g * f32x2{__builtin_amdgcn_rcpf(d[0]), __builtin_amdgcn_rcpf(d[1])}) * u;
  return cvtpk(y[0], y[1]);
}
DI void geglu16(const f32x16& g, const f32x16& u, u32x4& lo, u32x4& hi) {
#pragma unroll
  for (int i = 0; i < 4; i++) {
    lo[i] = geglu2(f32x2{g[2 * i], g[2 * i + 1]}, f32x2{u[2 * i], u[2 * i + 1]});
    hi[i] = geglu2(f32x2{g[8 + 2 * i], g[8 + 2 * i + 1]}, f32x2{u[8 + 2 * i], u[8 + 2 * i + 1]});
  }
}

DI void p1g4_prefill(unsigned long long& w0, unsigned long long& w1, unsigned vw0, unsigned vw1, unsigned st, unsigned ring) {
  asm volatile(
    "s_add_u32 m0, s45, 0\n"
    "s_nop 0\n"
    "global_load_lds_dwordx4 v188, s[40:41]\n"
    "s_add_u32 m0, s45, 1024\n"
    "s_nop 0\n"
    "global_load_lds_dwordx4 v189, s[40:41]\n"
    "s_add_u32 m0, s45, 2048\n"
    "s_nop 0\n"
    "global_load_lds_dwordx4 v188, s[42:43]\n"
    "s_add_u32 m0, s45, 3072\n"
    "s_nop 0\n"
    "global_load_lds_dwordx4 v189, s[42:43]\n"
    "s_add_u32 s40, s40, s44\n"
    "s_addc_u32 s41, s41, 0\n"
    "s_add_u32 s42, s42, s44\n"
    "s_addc_u32 s43, s43, 0\n"
    "s_add_u32 m0, s45, 4096\n"
    "s_nop 0\n"
    "global_load_lds_dwordx4 v188, s[40:41]\n"
    "s_add_u32 m0, s45, 5120\n"
    "s_nop 0\n"
    "global_load_lds_dwordx4 v189, s[40:41]\n"
    "s_add_u32 m0, s45, 6144\n"
    "s_nop 0\n"
    "global_load_lds_dwordx4 v188, s[42:43]\n"
    "s_add_u32 m0, s45, 7168\n"
    "s_nop 0\n"
    "global_load_lds_dwordx4 v189, s[42:43]\n"
    "s_add_u32 s40, s40, s44\n"
    "s_addc_u32 s41, s41, 0\n"
    "s_add_u32 s42, s42, s44\n"
    "s_addc_u32 s43, s43, 0\n"
    "s_add_u32 m0, s45, 8192\n"
    "s_nop 0\n"
    "global_load_lds_dwordx4 v188, s[40:41]\n"
    "s_add_u32 m0, s45, 9216\n"
    "s_nop 0\n"
    "global_load_lds_dwordx4 v189, s[40:41]\n"
    "s_add_u32 m0, s45, 10240\n"
    "s_nop 0\n"
    "global_load_lds_dwordx4 v188, s[42:43]\n"
    "s_add_u32 m0, s45, 11264\n"
    "s_nop 0\n"
    "global_load_lds_dwordx4 v189, s[42:43]\n"
    "s_add_u32 s40, s40, s44\n"
    "s_addc_u32 s41, s41, 0\n"
    "s_add_u32 s42, s42, s44\n"
    "s_addc_u32 s43, s43, 0\n"
    "s_add_u32 m0, s45, 12288\n"
    "s_nop 0\n"
    "global_load_lds_dwordx4 v188, s[40:41]\n"
    "s_add_u32 m0, s45, 13312\n"
    "s_nop 0\n"
    "global_load_lds_dwordx4 v189, s[40:41]\n"
    "s_add_u32 m0, s45, 14336\n"
    "s_nop 0\n"
    "global_load_lds_dwordx4 v188, s[42:43]\n"
    "s_add_u32 m0, s45, 15360\n"
    "s_nop 0\n"
    "global_load_lds_dwordx4 v189, s[42:43]\n"
    "s_add_u32 s40, s40, s44\n"
    "s_addc_u32 s41, s41, 0\n"
    "s_add_u32 s42, s42, s44\n"
    "s_addc_u32 s43, s43, 0\n"
    : "+{s[40:41]}"(w0), "+{s[42:43]}"(w1) : "{v188}"(vw0), "{v189}"(vw1), "{s44}"(st), "{s45}"(ring) : "scc", "memory");
}
DI void p1g4_loop(f32x16 (&acc)[8], unsigned long long w0, unsigned long long w1, unsigned vw0, unsigned vw1,
                  const unsigned (&va)[4], unsigned (&xb)[8], unsigned st, unsigned ring, unsigned wrap) {
  asm volatile(
    "v_mov_b32 v0, 0\n"
    "v_mov_b32 v1, 0\n"
    "v_mov_b32 v2, 0\n"
    "v_mov_b32 v3, 0\n"
    "v_mov_b32 v4, 0\n"
    "v_mov_b32 v5, 0\n"
    "v_mov_b32 v6, 0\n"
    "v_mov_b32 v7, 0\n"
    "v_mov_b32 v8, 0\n"
    "v_mov_b32 v9, 0\n"
    "v_mov_b32 v10, 0\n"
    "v_mov_b32 v11, 0\n"
    "v_mov_b32 v12, 0\n"
    "v_mov_b32 v13, 0\n"
    "v_mov_b32 v14, 0\n"
    "v_mov_b32 v15, 0\n"
    "v_mov_b32 v16, 0\n"
    "v_mov_b32 v17, 0\n"
    "v_mov_b32 v18, 0\n"
    "v_mov_b32 v19, 0\n"
    "v_mov_b32 v20, 0\n"
    "v_mov_b32 v21, 0\n"
    "v_mov_b32 v22, 0\n"
    "v_mov_b32 v23, 0\n"
    "v_mov_b32 v24, 0\n"
    "v_mov_b32 v25, 0\n"
    "v_mov_b32 v26, 0\n"
    "v_mov_b32 v27, 0\n"
    "v_mov_b32 v28, 0\n"
    "v_mov_b32 v29, 0\n"
    "v_mov_b32 v30, 0\n"
    "v_mov_b32 v31, 0\n"
    "v_mov_b32 v32, 0\n"
    "v_mov_b32 v33, 0\n"
    "v_mov_b32 v34, 0\n"
    "v_mov_b32 v35, 0\n"
    "v_mov_b32 v36, 0\n"
    "v_mov_b32 v37, 0\n"
    "v_mov_b32 v38, 0\n"
    "v_mov_b32 v39, 0\n"
    "v_mov_b32 v40, 0\n"
    "v_mov_b32 v41, 0\n"
    "v_mov_b32 v42, 0\n"
    "v_mov_b32 v43, 0\n"
    "v_mov_b32 v44, 0\n"
    "v_mov_b32 v45, 0\n"
    "v_mov_b32 v46, 0\n"
    "v_mov_b32 v47, 0\n"
    "v_mov_b32 v48, 0\n"
    "v_mov_b32 v49, 0\n"
    "v_mov_b32 v50, 0\n"
    "v_mov_b32 v51, 0\n"
    "v_mov_b32 v52, 0\n"
    "v_mov_b32 v53, 0\n"
    "v_mov_b32 v54, 0\n"
    "v_mov_b32 v55, 0\n"
    "v_mov_b32 v56, 0\n"
    "v_mov_b32 v57, 0\n"
    "v_mov_b32 v58, 0\n"
    "v_mov_b32 v59, 0\n"
    "v_mov_b32 v60, 0\n"
    "v_mov_b32 v61, 0\n"
    "v_mov_b32 v62, 0\n"
    "v_mov_b32 v63, 0\n"
    "v_mov_b32 v64, 0\n"
    "v_mov_b32 v65, 0\n"
    "v_mov_b32 v66, 0\n"
    "v_mov_b32 v67, 0\n"
    "v_mov_b32 v68, 0\n"
    "v_mov_b32 v69, 0\n"
    "v_mov_b32 v70, 0\n"
    "v_mov_b32 v71, 0\n"
    "v_mov_b32 v72, 0\n"
    "v_mov_b32 v73, 0\n"
    "v_mov_b32 v74, 0\n"
    "v_mov_b32 v75, 0\n"
    "v_mov_b32 v76, 0\n"
    "v_mov_b32 v77, 0\n"
    "v_mov_b32 v78, 0\n"
    "v_mov_b32 v79, 0\n"
    "v_mov_b32 v80, 0\n"
    "v_mov_b32 v81, 0\n"
    "v_mov_b32 v82, 0\n"
    "v_mov_b32 v83, 0\n"
    "v_mov_b32 v84, 0\n"
    "v_mov_b32 v85, 0\n"
    "v_mov_b32 v86, 0\n"
    "v_mov_b32 v87, 0\n"
    "v_mov_b32 v88, 0\n"
    "v_mov_b32 v89, 0\n"
    "v_mov_b32 v90, 0\n"
    "v_mov_b32 v91, 0\n"
    "v_mov_b32 v92, 0\n"
    "v_mov_b32 v93, 0\n"
    "v_mov_b32 v94, 0\n"
    "v_mov_b32 v95, 0\n"
    "v_mov_b32 v96, 0\n"
    "v_mov_b32 v97, 0\n"
    "v_mov_b32 v98, 0\n"
    "v_mov_b32 v99, 0\n"
    "v_mov_b32 v100, 0\n"
    "v_mov_b32 v101, 0\n"
    "v_mov_b32 v102, 0\n"
    "v_mov_b32 v103, 0\n"
    "v_mov_b32 v104, 0\n"
    "v_mov_b32 v105, 0\n"
    "v_mov_b32 v106, 0\n"
    "v_mov_b32 v107, 0\n"
    "v_mov_b32 v108, 0\n"
    "v_mov_b32 v109, 0\n"
    "v_mov_b32 v110, 0\n"
    "v_mov_b32 v111, 0\n"
    "v_mov_b32 v112, 0\n"
    "v_mov_b32 v113, 0\n"
    "v_mov_b32 v114, 0\n"
    "v_mov_b32 v115, 0\n"
    "v_mov_b32 v116, 0\n"
    "v_mov_b32 v117, 0\n"
    "v_mov_b32 v118, 0\n"
    "v_mov_b32 v119, 0\n"
    "v_mov_b32 v120, 0\n"
    "v_mov_b32 v121, 0\n"
    "v_mov_b32 v122, 0\n"
    "v_mov_b32 v123, 0\n"
    "v_mov_b32 v124, 0\n"
    "v_mov_b32 v125, 0\n"
    "v_mov_b32 v126, 0\n"
    "v_mov_b32 v127, 0\n"
    "s_setprio 0\n"
    "s_waitcnt vmcnt(12)\n"
    "ds_read_b128 v[144:147], v176 offset:0\n"
    "ds_read_b128 v[148:151], v176 offset:32768\n"
    "ds_read_b64_tr_b16 v[128:129], v184 offset:0\n"
    "ds_read_b64_tr_b16 v[130:131], v185 offset:0\n"
    "ds_read_b64_tr_b16 v[132:133], v186 offset:0\n"
    "ds_read_b64_tr_b16 v[134:135], v187 offset:0\n"
    "ds_read_b64_tr_b16 v[136:137], v184 offset:2048\n"
    "ds_read_b64_tr_b16 v[138:139], v185 offset:2048\n"
    "ds_read_b64_tr_b16 v[140:141], v186 offset:2048\n"
    "ds_read_b64_tr_b16 v[142:143], v187 offset:2048\n"
    "s_lshl_b32 s54, s44, 5\n"
    "s_mov_b32 s46, 3\n"
    "1:\n"
    "s_cmp_eq_u32 s51, 0\n"
    "s_cselect_b32 s52, s54, 0\n"
    "s_cselect_b32 s53, 0x400, 0\n"
    "s_sub_u32 s53, 0x100, s53\n"
    "s_sub_u32 s51, s51, 1\n"
    "s_waitcnt vmcnt(8) lgkmcnt(0)\n"
    "ds_read_b128 v[168:171], v177 offset:0\n"
    "ds_read_b128 v[172:175], v177 offset:32768\n"
    "ds_read_b64_tr_b16 v[152:153], v184 offset:4096\n"
    "ds_read_b64_tr_b16 v[154:155], v185 offset:4096\n"
    "ds_read_b64_tr_b16 v[156:157], v186 offset:4096\n"
    "ds_read_b64_tr_b16 v[158:159], v187 offset:4096\n"
    "ds_read_b64_tr_b16 v[160:161], v184 offset:6144\n"
    "ds_read_b64_tr_b16 v[162:163], v185 offset:6144\n"
    "ds_read_b64_tr_b16 v[164:165], v186 offset:6144\n"
    "ds_read_b64_tr_b16 v[166:167], v187 offset:6144\n"
    "v_mfma_f32_32x32x16_bf16 v[0:15], v[128:131], v[144:147], v[0:15]\n"
    "s_add_u32 m0, s45, 0\n"
    "s_nop 0\n"
    "global_load_lds_dwordx4 v188, s[40:41]\n"
    "s_add_u32 m0, s45, 1024\n"
    "s_nop 0\n"
    "global_load_lds_dwordx4 v189, s[40:41]\n"
    "s_add_u32 m0, s45, 2048\n"
    "s_nop 0\n"
    "global_load_lds_dwordx4 v188, s[42:43]\n"
    "s_add_u32 m0, s45, 3072\n"
    "s_nop 0\n"
    "global_load_lds_dwordx4 v189, s[42:43]\n"
    "s_add_u32 s40, s40, s44\n"
    "s_addc_u32 s41, s41, 0\n"
    "s_add_u32 s42, s42, s44\n"
    "s_addc_u32 s43, s43, 0\n"
    "v_mfma_f32_32x32x16_bf16 v[16:31], v[128:131], v[148:151], v[16:31]\n"
    "v_mfma_f32_32x32x16_bf16 v[32:47], v[132:135], v[144:147], v[32:47]\n"
    "v_mfma_f32_32x32x16_bf16 v[48:63], v[132:135], v[148:151], v[48:63]\n"
    "v_mfma_f32_32x32x16_bf16 v[64:79], v[136:139], v[144:147], v[64:79]\n"
    "v_mfma_f32_32x32x16_bf16 v[80:95], v[136:139], v[148:151], v[80:95]\n"
    "v_mfma_f32_32x32x16_bf16 v[96:111], v[140:143], v[144:147], v[96:111]\n"
    "v_mfma_f32_32x32x16_bf16 v[112:127], v[140:143], v[148:151], v[112:127]\n"
    "s_waitcnt vmcnt(8) lgkmcnt(0)\n"
    "ds_read_b128 v[144:147], v178 offset:0\n"
    "ds_read_b128 v[148:151], v178 offset:32768\n"
    "ds_read_b64_tr_b16 v[128:129], v184 offset:8192\n"
    "ds_read_b64_tr_b16 v[130:131], v185 offset:8192\n"
    "ds_read_b64_tr_b16 v[132:133], v186 offset:8192\n"
    "ds_read_b64_tr_b16 v[134:135], v187 offset:8192\n"
    "ds_read_b64_tr_b16 v[136:137], v184 offset:10240\n"
    "ds_read_b64_tr_b16 v[138:139], v185 offset:10240\n"
    "ds_read_b64_tr_b16 v[140:141], v186 offset:10240\n"
    "ds_read_b64_tr_b16 v[142:143], v187 offset:10240\n"
    "v_mfma_f32_32x32x16_bf16 v[0:15], v[152:155], v[168:171], v[0:15]\n"
    "s_add_u32 m0, s45, 4096\n"
    "s_nop 0\n"
    "global_load_lds_dwordx4 v188, s[40:41]\n"
    "s_add_u32 m0, s45, 5120\n"
    "s_nop 0\n"
    "global_load_lds_dwordx4 v189, s[40:41]\n"
    "s_add_u32 m0, s45, 6144\n"
    "s_nop 0\n"
    "global_load_lds_dwordx4 v188, s[42:43]\n"
    "s_add_u32 m0, s45, 7168\n"
    "s_nop 0\n"
    "global_load_lds_dwordx4 v189, s[42:43]\n"
    "s_add_u32 s40, s40, s44\n"
    "s_addc_u32 s41, s41, 0\n"
    "s_add_u32 s42, s42, s44\n"
    "s_addc_u32 s43, s43, 0\n"
    "v_mfma_f32_32x32x16_bf16 v[16:31], v[152:155], v[172:175], v[16:31]\n"
    "v_mfma_f32_32x32x16_bf16 v[32:47], v[156:159], v[168:171], v[32:47]\n"
    "v_mfma_f32_32x32x16_bf16 v[48:63], v[156:159], v[172:175], v[48:63]\n"
    "v_mfma_f32_32x32x16_bf16 v[64:79], v[160:163], v[168:171], v[64:79]\n"
    "v_mfma_f32_32x32x16_bf16 v[80:95], v[160:163], v[172:175], v[80:95]\n"
    "v_mfma_f32_32x32x16_bf16 v[96:111], v[164:167], v[168:171], v[96:111]\n"
    "v_mfma_f32_32x32x16_bf16 v[112:127], v[164:167], v[172:175], v[112:127]\n"
    "s_waitcnt vmcnt(8) lgkmcnt(0)\n"
    "ds_read_b128 v[168:171], v179 offset:0\n"
    "ds_read_b128 v[172:175], v179 offset:32768\n"
    "ds_read_b64_tr_b16 v[152:153], v184 offset:12288\n"
    "ds_read_b64_tr_b16 v[154:155], v185 offset:12288\n"
    "ds_read_b64_tr_b16 v[156:157], v186 offset:12288\n"
    "ds_read_b64_tr_b16 v[158:159], v187 offset:12288\n"
    "ds_read_b64_tr_b16 v[160:161], v184 offset:14336\n"
    "ds_read_b64_tr_b16 v[162:163], v185 offset:14336\n"
    "ds_read_b64_tr_b16 v[164:165], v186 offset:14336\n"
    "ds_read_b64_tr_b16 v[166:167], v187 offset:14336\n"
    "v_mfma_f32_32x32x16_bf16 v[0:15], v[128:131], v[144:147], v[0:15]\n"
    "s_add_u32 m0, s45, 8192\n"
    "s_nop 0\n"
    "global_load_lds_dwordx4 v188, s[40:41]\n"
    "s_add_u32 m0, s45, 9216\n"
    "s_nop 0\n"
    "global_load_lds_dwordx4 v189, s[40:41]\n"
    "s_add_u32 m0, s45, 10240\n"
    "s_nop 0\n"
    "global_load_lds_dwordx4 v188, s[42:43]\n"
    "s_add_u32 m0, s45, 11264\n"
    "s_nop 0\n"
    "global_load_lds_dwordx4 v189, s[42:43]\n"
    "s_add_u32 s40, s40, s44\n"
    "s_addc_u32 s41, s41, 0\n"
    "s_add_u32 s42, s42, s44\n"
    "s_addc_u32 s43, s43, 0\n"
    "v_mfma_f32_32x32x16_bf16 v[16:31], v[128:131], v[148:151], v[16:31]\n"
    "v_mfma_f32_32x32x16_bf16 v[32:47], v[132:135], v[144:147], v[32:47]\n"
    "v_mfma_f32_32x32x16_bf16 v[48:63], v[132:135], v[148:151], v[48:63]\n"
    "v_mfma_f32_32x32x16_bf16 v[64:79], v[136:139], v[144:147], v[64:79]\n"
    "v_mfma_f32_32x32x16_bf16 v[80:95], v[136:139], v[148:151], v[80:95]\n"
    "v_mfma_f32_32x32x16_bf16 v[96:111], v[140:143], v[144:147], v[96:111]\n"
    "v_mfma_f32_32x32x16_bf16 v[112:127], v[140:143], v[148:151], v[112:127]\n"
    "s_waitcnt vmcnt(8) lgkmcnt(0)\n"
    "ds_read_b128 v[144:147], v180 offset:0\n"
    "ds_read_b128 v[148:151], v180 offset:32768\n"
    "ds_read_b64_tr_b16 v[128:129], v184 offset:0\n"
    "ds_read_b64_tr_b16 v[130:131], v185 offset:0\n"
    "ds_read_b64_tr_b16 v[132:133], v186 offset:0\n"
    "ds_read_b64_tr_b16 v[134:135], v187 offset:0\n"
    "ds_read_b64_tr_b16 v[136:137], v184 offset:2048\n"
    "ds_read_b64_tr_b16 v[138:139], v185 offset:2048\n"
    "ds_read_b64_tr_b16 v[140:141], v186 offset:2048\n"
    "ds_read_b64_tr_b16 v[142:143], v187 offset:2048\n"
    "v_mfma_f32_32x32x16_bf16 v[0:15], v[152:155], v[168:171], v[0:15]\n"
    "s_add_u32 m0, s45, 12288\n"
    "s_nop 0\n"
    "global_load_lds_dwordx4 v188, s[40:41]\n"
    "s_add_u32 m0, s45, 13312\n"
    "s_nop 0\n"
    "global_load_lds_dwordx4 v189, s[40:41]\n"
    "s_add_u32 m0, s45, 14336\n"
    "s_nop 0\n"
    "global_load_lds_dwordx4 v188, s[42:43]\n"
    "s_add_u32 m0, s45, 15360\n"
    "s_nop 0\n"
    "global_load_lds_dwordx4 v189, s[42:43]\n"
    "s_add_u32 s40, s40, s44\n"
    "s_addc_u32 s41, s41, 0\n"
    "s_add_u32 s42, s42, s44\n"
    "s_addc_u32 s43, s43, 0\n"
    "v_mfma_f32_32x32x16_bf16 v[16:31], v[152:155], v[172:175], v[16:31]\n"
    "v_mfma_f32_32x32x16_bf16 v[32:47], v[156:159], v[168:171], v[32:47]\n"
    "v_mfma_f32_32x32x16_bf16 v[48:63], v[156:159], v[172:175], v[48:63]\n"
    "v_mfma_f32_32x32x16_bf16 v[64:79], v[160:163], v[168:171], v[64:79]\n"
    "v_mfma_f32_32x32x16_bf16 v[80:95], v[160:163], v[172:175], v[80:95]\n"
    "v_mfma_f32_32x32x16_bf16 v[96:111], v[164:167], v[168:171], v[96:111]\n"
    "v_mfma_f32_32x32x16_bf16 v[112:127], v[164:167], v[172:175], v[112:127]\n"
    "s_sub_u32 s40, s40, s52\n"
    "s_subb_u32 s41, s41, 0\n"
    "s_sub_u32 s42, s42, s52\n"
    "s_subb_u32 s43, s43, 0\n"
    "s_waitcnt vmcnt(8) lgkmcnt(0)\n"
    "ds_read_b128 v[168:171], v181 offset:0\n"
    "ds_read_b128 v[172:175], v181 offset:32768\n"
    "ds_read_b64_tr_b16 v[152:153], v184 offset:4096\n"
    "ds_read_b64_tr_b16 v[154:155], v185 offset:4096\n"
    "ds_read_b64_tr_b16 v[156:157], v186 offset:4096\n"
    "ds_read_b64_tr_b16 v[158:159], v187 offset:4096\n"
    "ds_read_b64_tr_b16 v[160:161], v184 offset:6144\n"
    "ds_read_b64_tr_b16 v[162:163], v185 offset:6144\n"
    "ds_read_b64_tr_b16 v[164:165], v186 offset:6144\n"
    "ds_read_b64_tr_b16 v[166:167], v187 offset:6144\n"
    "v_mfma_f32_32x32x16_bf16 v[0:15], v[128:131], v[144:147], v[0:15]\n"
    "s_add_u32 m0, s45, 0\n"
    "s_nop 0\n"
    "global_load_lds_dwordx4 v188, s[40:41]\n"
    "s_add_u32 m0, s45, 1024\n"
    "s_nop 0\n"
    "global_load_lds_dwordx4 v189, s[40:41]\n"
    "s_add_u32 m0, s45, 2048\n"
    "s_nop 0\n"
    "global_load_lds_dwordx4 v188, s[42:43]\n"
    "s_add_u32 m0, s45, 3072\n"
    "s_nop 0\n"
    "global_load_lds_dwordx4 v189, s[42:43]\n"
    "s_add_u32 s40, s40, s44\n"
    "s_addc_u32 s41, s41, 0\n"
    "s_add_u32 s42, s42, s44\n"
    "s_addc_u32 s43, s43, 0\n"
    "v_mfma_f32_32x32x16_bf16 v[16:31], v[128:131], v[148:151], v[16:31]\n"
    "v_mfma_f32_32x32x16_bf16 v[32:47], v[132:135], v[144:147], v[32:47]\n"
    "v_mfma_f32_32x32x16_bf16 v[48:63], v[132:135], v[148:151], v[48:63]\n"
    "v_mfma_f32_32x32x16_bf16 v[64:79], v[136:139], v[144:147], v[64:79]\n"
    "v_mfma_f32_32x32x16_bf16 v[80:95], v[136:139], v[148:151], v[80:95]\n"
    "v_mfma_f32_32x32x16_bf16 v[96:111], v[140:143], v[144:147], v[96:111]\n"
    "v_mfma_f32_32x32x16_bf16 v[112:127], v[140:143], v[148:151], v[112:127]\n"
    "s_waitcnt vmcnt(8) lgkmcnt(0)\n"
    "ds_read_b128 v[144:147], v182 offset:0\n"
    "ds_read_b128 v[148:151], v182 offset:32768\n"
    "ds_read_b64_tr_b16 v[128:129], v184 offset:8192\n"
    "ds_read_b64_tr_b16 v[130:131], v185 offset:8192\n"
    "ds_read_b64_tr_b16 v[132:133], v186 offset:8192\n"
    "ds_read_b64_tr_b16 v[134:135], v187 offset:8192\n"
    "ds_read_b64_tr_b16 v[136:137], v184 offset:10240\n"
    "ds_read_b64_tr_b16 v[138:139], v185 offset:10240\n"
    "ds_read_b64_tr_b16 v[140:141], v186 offset:10240\n"
    "ds_read_b64_tr_b16 v[142:143], v187 offset:10240\n"
    "v_mfma_f32_32x32x16_bf16 v[0:15], v[152:155], v[168:171], v[0:15]\n"
    "s_add_u32 m0, s45, 4096\n"
    "s_nop 0\n"
    "global_load_lds_dwordx4 v188, s[40:41]\n"
    "s_add_u32 m0, s45, 5120\n"
    "s_nop 0\n"
    "global_load_lds_dwordx4 v189, s[40:41]\n"
    "s_add_u32 m0, s45, 6144\n"
    "s_nop 0\n"
    "global_load_lds_dwordx4 v188, s[42:43]\n"
    "s_add_u32 m0, s45, 7168\n"
    "s_nop 0\n"
    "global_load_lds_dwordx4 v189, s[42:43]\n"
    "s_add_u32 s40, s40, s44\n"
    "s_addc_u32 s41, s41, 0\n"
    "s_add_u32 s42, s42, s44\n"
    "s_addc_u32 s43, s43, 0\n"
    "v_mfma_f32_32x32x16_bf16 v[16:31], v[152:155], v[172:175], v[16:31]\n"
    "v_mfma_f32_32x32x16_bf16 v[32:47], v[156:159], v[168:171], v[32:47]\n"
    "v_mfma_f32_32x32x16_bf16 v[48:63], v[156:159], v[172:175], v[48:63]\n"
    "v_mfma_f32_32x32x16_bf16 v[64:79], v[160:163], v[168:171], v[64:79]\n"
    "v_mfma_f32_32x32x16_bf16 v[80:95], v[160:163], v[172:175], v[80:95]\n"
    "v_mfma_f32_32x32x16_bf16 v[96:111], v[164:167], v[168:171], v[96:111]\n"
    "v_mfma_f32_32x32x16_bf16 v[112:127], v[164:167], v[172:175], v[112:127]\n"
    "s_waitcnt vmcnt(8) lgkmcnt(0)\n"
    "ds_read_b128 v[168:171], v183 offset:0\n"
    "ds_read_b128 v[172:175], v183 offset:32768\n"
    "ds_read_b64_tr_b16 v[152:153], v184 offset:12288\n"
    "ds_read_b64_tr_b16 v[154:155], v185 offset:12288\n"
    "ds_read_b64_tr_b16 v[156:157], v186 offset:12288\n"
    "ds_read_b64_tr_b16 v[158:159], v187 offset:12288\n"
    "ds_read_b64_tr_b16 v[160:161], v184 offset:14336\n"
    "ds_read_b64_tr_b16 v[162:163], v185 offset:14336\n"
    "ds_read_b64_tr_b16 v[164:165], v186 offset:14336\n"
    "ds_read_b64_tr_b16 v[166:167], v187 offset:14336\n"
    "v_mfma_f32_32x32x16_bf16 v[0:15], v[128:131], v[144:147], v[0:15]\n"
    "s_add_u32 m0, s45, 8192\n"
    "s_nop 0\n"
    "global_load_lds_dwordx4 v188, s[40:41]\n"
    "s_add_u32 m0, s45, 9216\n"
    "s_nop 0\n"
    "global_load_lds_dwordx4 v189, s[40:41]\n"
    "s_add_u32 m0, s45, 10240\n"
    "s_nop 0\n"
    "global_load_lds_dwordx4 v188, s[42:43]\n"
    "s_add_u32 m0, s45, 11264\n"
    "s_nop 0\n"
    "global_load_lds_dwordx4 v189, s[42:43]\n"
    "s_add_u32 s40, s40, s44\n"
    "s_addc_u32 s41, s41, 0\n"
    "s_add_u32 s42, s42, s44\n"
    "s_addc_u32 s43, s43, 0\n"
    "v_mfma_f32_32x32x16_bf16 v[16:31], v[128:131], v[148:151], v[16:31]\n"
    "v_mfma_f32_32x32x16_bf16 v[32:47], v[132:135], v[144:147], v[32:47]\n"
    "v_mfma_f32_32x32x16_bf16 v[48:63], v[132:135], v[148:151], v[48:63]\n"
    "v_mfma_f32_32x32x16_bf16 v[64:79], v[136:139], v[144:147], v[64:79]\n"
    "v_mfma_f32_32x32x16_bf16 v[80:95], v[136:139], v[148:151], v[80:95]\n"
    "v_mfma_f32_32x32x16_bf16 v[96:111], v[140:143], v[144:147], v[96:111]\n"
    "v_mfma_f32_32x32x16_bf16 v[112:127], v[140:143], v[148:151], v[112:127]\n"
    "s_waitcnt vmcnt(8) lgkmcnt(0)\n"
    "s_setprio 3\n"
    "v_add_u32 v176, s53, v176\n"
    "v_add_u32 v177, s53, v177\n"
    "v_add_u32 v178, s53, v178\n"
    "v_add_u32 v179, s53, v179\n"
    "v_add_u32 v180, s53, v180\n"
    "v_add_u32 v181, s53, v181\n"
    "v_add_u32 v182, s53, v182\n"
    "v_add_u32 v183, s53, v183\n"
    "s_setprio 0\n"
    "ds_read_b128 v[144:147], v176 offset:0\n"
    "ds_read_b128 v[148:151], v176 offset:32768\n"
    "ds_read_b64_tr_b16 v[128:129], v184 offset:0\n"
    "ds_read_b64_tr_b16 v[130:131], v185 offset:0\n"
    "ds_read_b64_tr_b16 v[132:133], v186 offset:0\n"
    "ds_read_b64_tr_b16 v[134:135], v187 offset:0\n"
    "ds_read_b64_tr_b16 v[136:137], v184 offset:2048\n"
    "ds_read_b64_tr_b16 v[138:139], v185 offset:2048\n"
    "ds_read_b64_tr_b16 v[140:141], v186 offset:2048\n"
    "ds_read_b64_tr_b16 v[142:143], v187 offset:2048\n"
    "v_mfma_f32_32x32x16_bf16 v[0:15], v[152:155], v[168:171], v[0:15]\n"
    "s_add_u32 m0, s45, 12288\n"
    "s_nop 0\n"
    "global_load_lds_dwordx4 v188, s[40:41]\n"
    "s_add_u32 m0, s45, 13312\n"
    "s_nop 0\n"
    "global_load_lds_dwordx4 v189, s[40:41]\n"
    "s_add_u32 m0, s45, 14336\n"
    "s_nop 0\n"
    "global_load_lds_dwordx4 v188, s[42:43]\n"
    "s_add_u32 m0, s45, 15360\n"
    "s_nop 0\n"
    "global_load_lds_dwordx4 v189, s[42:43]\n"
    "s_add_u32 s40, s40, s44\n"
    "s_addc_u32 s41, s41, 0\n"
    "s_add_u32 s42, s42, s44\n"
    "s_addc_u32 s43, s43, 0\n"
    "v_mfma_f32_32x32x16_bf16 v[16:31], v[152:155], v[172:175], v[16:31]\n"
    "v_mfma_f32_32x32x16_bf16 v[32:47], v[156:159], v[168:171], v[32:47]\n"
    "v_mfma_f32_32x32x16_bf16 v[48:63], v[156:159], v[172:175], v[48:63]\n"
    "v_mfma_f32_32x32x16_bf16 v[64:79], v[160:163], v[168:171], v[64:79]\n"
    "v_mfma_f32_32x32x16_bf16 v[80:95], v[160:163], v[172:175], v[80:95]\n"
    "v_mfma_f32_32x32x16_bf16 v[96:111], v[164:167], v[168:171], v[96:111]\n"
    "v_mfma_f32_32x32x16_bf16 v[112:127], v[164:167], v[172:175], v[112:127]\n"
    "s_sub_u32 s46, s46, 1\n"
    "s_cmp_lg_u32 s46, 0\n"
    "s_cbranch_scc1 1b\n"
    "s_waitcnt vmcnt(8) lgkmcnt(0)\n"
    "ds_read_b128 v[168:171], v177 offset:0\n"
    "ds_read_b128 v[172:175], v177 offset:32768\n"
    "ds_read_b64_tr_b16 v[152:153], v184 offset:4096\n"
    "ds_read_b64_tr_b16 v[154:155], v185 offset:4096\n"
    "ds_read_b64_tr_b16 v[156:157], v186 offset:4096\n"
    "ds_read_b64_tr_b16 v[158:159], v187 offset:4096\n"
    "ds_read_b64_tr_b16 v[160:161], v184 offset:6144\n"
    "ds_read_b64_tr_b16 v[162:163], v185 offset:6144\n"
    "ds_read_b64_tr_b16 v[164:165], v186 offset:6144\n"
    "ds_read_b64_tr_b16 v[166:167], v187 offset:6144\n"
    "v_mfma_f32_32x32x16_bf16 v[0:15], v[128:131], v[144:147], v[0:15]\n"
    "s_add_u32 m0, s45, 0\n"
    "s_nop 0\n"
    "global_load_lds_dwordx4 v188, s[40:41]\n"
    "s_add_u32 m0, s45, 1024\n"
    "s_nop 0\n"
    "global_load_lds_dwordx4 v189, s[40:41]\n"
    "s_add_u32 m0, s45, 2048\n"
    "s_nop 0\n"
    "global_load_lds_dwordx4 v188, s[42:43]\n"
    "s_add_u32 m0, s45, 3072\n"
    "s_nop 0\n"
    "global_load_lds_dwordx4 v189, s[42:43]\n"
    "s_add_u32 s40, s40, s44\n"
    "s_addc_u32 s41, s41, 0\n"
    "s_add_u32 s42, s42, s44\n"
    "s_addc_u32 s43, s43, 0\n"
    "v_mfma_f32_32x32x16_bf16 v[16:31], v[128:131], v[148:151], v[16:31]\n"
    "v_mfma_f32_32x32x16_bf16 v[32:47], v[132:135], v[144:147], v[32:47]\n"
    "v_mfma_f32_32x32x16_bf16 v[48:63], v[132:135], v[148:151], v[48:63]\n"
    "v_mfma_f32_32x32x16_bf16 v[64:79], v[136:139], v[144:147], v[64:79]\n"
    "v_mfma_f32_32x32x16_bf16 v[80:95], v[136:139], v[148:151], v[80:95]\n"
    "v_mfma_f32_32x32x16_bf16 v[96:111], v[140:143], v[144:147], v[96:111]\n"
    "v_mfma_f32_32x32x16_bf16 v[112:127], v[140:143], v[148:151], v[112:127]\n"
    "s_waitcnt vmcnt(8) lgkmcnt(0)\n"
    "ds_read_b128 v[144:147], v178 offset:0\n"
    "ds_read_b128 v[148:151], v178 offset:32768\n"
    "ds_read_b64_tr_b16 v[128:129], v184 offset:8192\n"
    "ds_read_b64_tr_b16 v[130:131], v185 offset:8192\n"
    "ds_read_b64_tr_b16 v[132:133], v186 offset:8192\n"
    "ds_read_b64_tr_b16 v[134:135], v187 offset:8192\n"
    "ds_read_b64_tr_b16 v[136:137], v184 offset:10240\n"
    "ds_read_b64_tr_b16 v[138:139], v185 offset:10240\n"
    "ds_read_b64_tr_b16 v[140:141], v186 offset:10240\n"
    "ds_read_b64_tr_b16 v[142:143], v187 offset:10240\n"
    "v_mfma_f32_32x32x16_bf16 v[0:15], v[152:155], v[168:171], v[0:15]\n"
    "s_add_u32 m0, s45, 4096\n"
    "s_nop 0\n"
    "global_load_lds_dwordx4 v188, s[40:41]\n"
    "s_add_u32 m0, s45, 5120\n"
    "s_nop 0\n"
    "global_load_lds_dwordx4 v189, s[40:41]\n"
    "s_add_u32 m0, s45, 6144\n"
    "s_nop 0\n"
    "global_load_lds_dwordx4 v188, s[42:43]\n"
    "s_add_u32 m0, s45, 7168\n"
    "s_nop 0\n"
    "global_load_lds_dwordx4 v189, s[42:43]\n"
    "s_add_u32 s40, s40, s44\n"
    "s_addc_u32 s41, s41, 0\n"
    "s_add_u32 s42, s42, s44\n"
    "s_addc_u32 s43, s43, 0\n"
    "v_mfma_f32_32x32x16_bf16 v[16:31], v[152:155], v[172:175], v[16:31]\n"
    "v_mfma_f32_32x32x16_bf16 v[32:47], v[156:159], v[168:171], v[32:47]\n"
    "v_mfma_f32_32x32x16_bf16 v[48:63], v[156:159], v[172:175], v[48:63]\n"
    "v_mfma_f32_32x32x16_bf16 v[64:79], v[160:163], v[168:171], v[64:79]\n"
    "v_mfma_f32_32x32x16_bf16 v[80:95], v[160:163], v[172:175], v[80:95]\n"
    "v_mfma_f32_32x32x16_bf16 v[96:111], v[164:167], v[168:171], v[96:111]\n"
    "v_mfma_f32_32x32x16_bf16 v[112:127], v[164:167], v[172:175], v[112:127]\n"
    "s_waitcnt vmcnt(8) lgkmcnt(0)\n"
    "ds_read_b128 v[168:171], v179 offset:0\n"
    "ds_read_b128 v[172:175], v179 offset:32768\n"
    "ds_read_b64_tr_b16 v[152:153], v184 offset:12288\n"
    "ds_read_b64_tr_b16 v[154:155], v185 offset:12288\n"
    "ds_read_b64_tr_b16 v[156:157], v186 offset:12288\n"
    "ds_read_b64_tr_b16 v[158:159], v187 offset:12288\n"
    "ds_read_b64_tr_b16 v[160:161], v184 offset:14336\n"
    "ds_read_b64_tr_b16 v[162:163], v185 offset:14336\n"
    "ds_read_b64_tr_b16 v[164:165], v186 offset:14336\n"
    "ds_read_b64_tr_b16 v[166:167], v187 offset:14336\n"
    "v_mfma_f32_32x32x16_bf16 v[0:15], v[128:131], v[144:147], v[0:15]\n"
    "s_add_u32 m0, s45, 8192\n"
    "s_nop 0\n"
    "global_load_lds_dwordx4 v188, s[40:41]\n"
    "s_add_u32 m0, s45, 9216\n"
    "s_nop 0\n"
    "global_load_lds_dwordx4 v189, s[40:41]\n"
    "s_add_u32 m0, s45, 10240\n"
    "s_nop 0\n"
    "global_load_lds_dwordx4 v188, s[42:43]\n"
    "s_add_u32 m0, s45, 11264\n"
    "s_nop 0\n"
    "global_load_lds_dwordx4 v189, s[42:43]\n"
    "s_add_u32 s40, s40, s44\n"
    "s_addc_u32 s41, s41, 0\n"
    "s_add_u32 s42, s42, s44\n"
    "s_addc_u32 s43, s43, 0\n"
    "v_mfma_f32_32x32x16_bf16 v[16:31], v[128:131], v[148:151], v[16:31]\n"
    "v_mfma_f32_32x32x16_bf16 v[32:47], v[132:135], v[144:147], v[32:47]\n"
    "v_mfma_f32_32x32x16_bf16 v[48:63], v[132:135], v[148:151], v[48:63]\n"
    "v_mfma_f32_32x32x16_bf16 v[64:79], v[136:139], v[144:147], v[64:79]\n"
    "v_mfma_f32_32x32x16_bf16 v[80:95], v[136:139], v[148:151], v[80:95]\n"
    "v_mfma_f32_32x32x16_bf16 v[96:111], v[140:143], v[144:147], v[96:111]\n"
    "v_mfma_f32_32x32x16_bf16 v[112:127], v[140:143], v[148:151], v[112:127]\n"
    "s_waitcnt vmcnt(8) lgkmcnt(0)\n"
    "ds_read_b128 v[144:147], v180 offset:0\n"
    "ds_read_b128 v[148:151], v180 offset:32768\n"
    "ds_read_b64_tr_b16 v[128:129], v184 offset:0\n"
    "ds_read_b64_tr_b16 v[130:131], v185 offset:0\n"
    "ds_read_b64_tr_b16 v[132:133], v186 offset:0\n"
    "ds_read_b64_tr_b16 v[134:135], v187 offset:0\n"
    "ds_read_b64_tr_b16 v[136:137], v184 offset:2048\n"
    "ds_read_b64_tr_b16 v[138:139], v185 offset:2048\n"
    "ds_read_b64_tr_b16 v[140:141], v186 offset:2048\n"
    "ds_read_b64_tr_b16 v[142:143], v187 offset:2048\n"
    "v_mfma_f32_32x32x16_bf16 v[0:15], v[152:155], v[168:171], v[0:15]\n"
    "s_add_u32 m0, s45, 12288\n"
    "s_nop 0\n"
    "global_load_lds_dwordx4 v188, s[40:41]\n"
    "s_add_u32 m0, s45, 13312\n"
    "s_nop 0\n"
    "global_load_lds_dwordx4 v189, s[40:41]\n"
    "s_add_u32 m0, s45, 14336\n"
    "s_nop 0\n"
    "global_load_lds_dwordx4 v188, s[42:43]\n"
    "s_add_u32 m0, s45, 15360\n"
    "s_nop 0\n"
    "global_load_lds_dwordx4 v189, s[42:43]\n"
    "s_add_u32 s40, s40, s44\n"
    "s_addc_u32 s41, s41, 0\n"
    "s_add_u32 s42, s42, s44\n"
    "s_addc_u32 s43, s43, 0\n"
    "v_mfma_f32_32x32x16_bf16 v[16:31], v[152:155], v[172:175], v[16:31]\n"
    "v_mfma_f32_32x32x16_bf16 v[32:47], v[156:159], v[168:171], v[32:47]\n"
    "v_mfma_f32_32x32x16_bf16 v[48:63], v[156:159], v[172:175], v[48:63]\n"
    "v_mfma_f32_32x32x16_bf16 v[64:79], v[160:163], v[168:171], v[64:79]\n"
    "v_mfma_f32_32x32x16_bf16 v[80:95], v[160:163], v[172:175], v[80:95]\n"
    "v_mfma_f32_32x32x16_bf16 v[96:111], v[164:167], v[168:171], v[96:111]\n"
    "v_mfma_f32_32x32x16_bf16 v[112:127], v[164:167], v[172:175], v[112:127]\n"
    "s_waitcnt vmcnt(8) lgkmcnt(0)\n"
    "ds_read_b128 v[168:171], v181 offset:0\n"
    "ds_read_b128 v[172:175], v181 offset:32768\n"
    "ds_read_b64_tr_b16 v[152:153], v184 offset:4096\n"
    "ds_read_b64_tr_b16 v[154:155], v185 offset:4096\n"
    "ds_read_b64_tr_b16 v[156:157], v186 offset:4096\n"
    "ds_read_b64_tr_b16 v[158:159], v187 offset:4096\n"
    "ds_read_b64_tr_b16 v[160:161], v184 offset:6144\n"
    "ds_read_b64_tr_b16 v[162:163], v185 offset:6144\n"
    "ds_read_b64_tr_b16 v[164:165], v186 offset:6144\n"
    "ds_read_b64_tr_b16 v[166:167], v187 offset:6144\n"
    "v_mfma_f32_32x32x16_bf16 v[0:15], v[128:131], v[144:147], v[0:15]\n"
    "v_mfma_f32_32x32x16_bf16 v[16:31], v[128:131], v[148:151], v[16:31]\n"
    "v_mfma_f32_32x32x16_bf16 v[32:47], v[132:135], v[144:147], v[32:47]\n"
    "v_mfma_f32_32x32x16_bf16 v[48:63], v[132:135], v[148:151], v[48:63]\n"
    "v_mfma_f32_32x32x16_bf16 v[64:79], v[136:139], v[144:147], v[64:79]\n"
    "v_mfma_f32_32x32x16_bf16 v[80:95], v[136:139], v[148:151], v[80:95]\n"
    "v_mfma_f32_32x32x16_bf16 v[96:111], v[140:143], v[144:147], v[96:111]\n"
    "v_mfma_f32_32x32x16_bf16 v[112:127], v[140:143], v[148:151], v[112:127]\n"
    "s_waitcnt vmcnt(4) lgkmcnt(0)\n"
    "ds_read_b128 v[144:147], v182 offset:0\n"
    "ds_read_b128 v[148:151], v182 offset:32768\n"
    "ds_read_b64_tr_b16 v[128:129], v184 offset:8192\n"
    "ds_read_b64_tr_b16 v[130:131], v185 offset:8192\n"
    "ds_read_b64_tr_b16 v[132:133], v186 offset:8192\n"
    "ds_read_b64_tr_b16 v[134:135], v187 offset:8192\n"
    "ds_read_b64_tr_b16 v[136:137], v184 offset:10240\n"
    "ds_read_b64_tr_b16 v[138:139], v185 offset:10240\n"
    "ds_read_b64_tr_b16 v[140:141], v186 offset:10240\n"
    "ds_read_b64_tr_b16 v[142:143], v187 offset:10240\n"
    "v_mfma_f32_32x32x16_bf16 v[0:15], v[152:155], v[168:171], v[0:15]\n"
    "v_mfma_f32_32x32x16_bf16 v[16:31], v[152:155], v[172:175], v[16:31]\n"
    "v_mfma_f32_32x32x16_bf16 v[32:47], v[156:159], v[168:171], v[32:47]\n"
    "v_mfma_f32_32x32x16_bf16 v[48:63], v[156:159], v[172:175], v[48:63]\n"
    "v_mfma_f32_32x32x16_bf16 v[64:79], v[160:163], v[168:171], v[64:79]\n"
    "v_mfma_f32_32x32x16_bf16 v[80:95], v[160:163], v[172:175], v[80:95]\n"
    "v_mfma_f32_32x32x16_bf16 v[96:111], v[164:167], v[168:171], v[96:111]\n"
    "v_mfma_f32_32x32x16_bf16 v[112:127], v[164:167], v[172:175], v[112:127]\n"
    "s_waitcnt vmcnt(0) lgkmcnt(0)\n"
    "ds_read_b128 v[168:171], v183 offset:0\n"
    "ds_read_b128 v[172:175], v183 offset:32768\n"
    "ds_read_b64_tr_b16 v[152:153], v184 offset:12288\n"
    "ds_read_b64_tr_b16 v[154:155], v185 offset:12288\n"
    "ds_read_b64_tr_b16 v[156:157], v186 offset:12288\n"
    "ds_read_b64_tr_b16 v[158:159], v187 offset:12288\n"
    "ds_read_b64_tr_b16 v[160:161], v184 offset:14336\n"
    "ds_read_b64_tr_b16 v[162:163], v185 offset:14336\n"
    "ds_read_b64_tr_b16 v[164:165], v186 offset:14336\n"
    "ds_read_b64_tr_b16 v[166:167], v187 offset:14336\n"
    "v_mfma_f32_32x32x16_bf16 v[0:15], v[128:131], v[144:147], v[0:15]\n"
    "v_mfma_f32_32x32x16_bf16 v[16:31], v[128:131], v[148:151], v[16:31]\n"
    "v_mfma_f32_32x32x16_bf16 v[32:47], v[132:135], v[144:147], v[32:47]\n"
    "v_mfma_f32_32x32x16_bf16 v[48:63], v[132:135], v[148:151], v[48:63]\n"
    "v_mfma_f32_32x32x16_bf16 v[64:79], v[136:139], v[144:147], v[64:79]\n"
    "v_mfma_f32_32x32x16_bf16 v[80:95], v[136:139], v[148:151], v[80:95]\n"
    "v_mfma_f32_32x32x16_bf16 v[96:111], v[140:143], v[144:147], v[96:111]\n"
    "v_mfma_f32_32x32x16_bf16 v[112:127], v[140:143], v[148:151], v[112:127]\n"
    "s_waitcnt lgkmcnt(0)\n"
    "v_mfma_f32_32x32x16_bf16 v[0:15], v[152:155], v[168:171], v[0:15]\n"
    "v_mfma_f32_32x32x16_bf16 v[16:31], v[152:155], v[172:175], v[16:31]\n"
    "v_mfma_f32_32x32x16_bf16 v[32:47], v[156:159], v[168:171], v[32:47]\n"
    "v_mfma_f32_32x32x16_bf16 v[48:63], v[156:159], v[172:175], v[48:63]\n"
    "v_mfma_f32_32x32x16_bf16 v[64:79], v[160:163], v[168:171], v[64:79]\n"
    "v_mfma_f32_32x32x16_bf16 v[80:95], v[160:163], v[172:175], v[80:95]\n"
    "v_mfma_f32_32x32x16_bf16 v[96:111], v[164:167], v[168:171], v[96:111]\n"
    "v_mfma_f32_32x32x16_bf16 v[112:127], v[164:167], v[172:175], v[112:127]\n"
    "s_setprio 2\n"
    "s_nop 7\n"
    "s_nop 7\n"
    : "={v[0:15]}"(acc[0]), "={v[16:31]}"(acc[1]), "={v[32:47]}"(acc[2]), "={v[48:63]}"(acc[3]), "={v[64:79]}"(acc[4]), "={v[80:95]}"(acc[5]), "={v[96:111]}"(acc[6]), "={v[112:127]}"(acc[7]), "+{s[40:41]}"(w0), "+{s[42:43]}"(w1), "+{v176}"(xb[0]), "+{v177}"(xb[1]), "+{v178}"(xb[2]), "+{v179}"(xb[3]), "+{v180}"(xb[4]), "+{v181}"(xb[5]), "+{v182}"(xb[6]), "+{v183}"(xb[7]), "+{s51}"(wrap)
    : "{v184}"(va[0]), "{v185}"(va[1]), "{v186}"(va[2]), "{v187}"(va[3]), "{v188}"(vw0), "{v189}"(vw1), "{s44}"(st), "{s45}"(ring)
    : "v128", "v129", "v130", "v131", "v132", "v133", "v134", "v135", "v136", "v137", "v138", "v139", "v140", "v141", "v142", "v143", "v144", "v145", "v146", "v147", "v148", "v149", "v150", "v151", "v152", "v153", "v154", "v155", "v156", "v157", "v158", "v159", "v160", "v161", "v162", "v163", "v164", "v165", "v166", "v167", "v168", "v169", "v170", "v171", "v172", "v173", "v174", "v175", "s46", "s52", "s53", "s54", "scc", "memory");
}
DI void p1g2_prefill(unsigned long long& w0, unsigned long long& w1, unsigned vw0, unsigned vw1, unsigned st, unsigned ring) {
  asm volatile(
    "s_add_u32 m0, s45, 0\n"
    "s_nop 0\n"
    "global_load_lds_dwordx4 v108, s[40:41]\n"
    "s_add_u32 m0, s45, 1024\n"
    "s_nop 0\n"
    "global_load_lds_dwordx4 v108, s[42:43]\n"
    "s_add_u32 s40, s40, s44\n"
    "s_addc_u32 s41, s41, 0\n"
    "s_add_u32 s42, s42, s44\n"
    "s_addc_u32 s43, s43, 0\n"
    "s_add_u32 m0, s45, 2048\n"
    "s_nop 0\n"
    "global_load_lds_dwordx4 v108, s[40:41]\n"
    "s_add_u32 m0, s45, 3072\n"
    "s_nop 0\n"
    "global_load_lds_dwordx4 v108, s[42:43]\n"
    "s_add_u32 s40, s40, s44\n"
    "s_addc_u32 s41, s41, 0\n"
    "s_add_u32 s42, s42, s44\n"
    "s_addc_u32 s43, s43, 0\n"
    "s_add_u32 m0, s45, 4096\n"
    "s_nop 0\n"
    "global_load_lds_dwordx4 v108, s[40:41]\n"
    "s_add_u32 m0, s45, 5120\n"
    "s_nop 0\n"
    "global_load_lds_dwordx4 v108, s[42:43]\n"
    "s_add_u32 s40, s40, s44\n"
    "s_addc_u32 s41, s41, 0\n"
    "s_add_u32 s42, s42, s44\n"
    "s_addc_u32 s43, s43, 0\n"
    "s_add_u32 m0, s45, 6144\n"
    "s_nop 0\n"
    "global_load_lds_dwordx4 v108, s[40:41]\n"
    "s_add_u32 m0, s45, 7168\n"
    "s_nop 0\n"
    "global_load_lds_dwordx4 v108, s[42:43]\n"
    "s_add_u32 s40, s40, s44\n"
    "s_addc_u32 s41, s41, 0\n"
    "s_add_u32 s42, s42, s44\n"
    "s_addc_u32 s43, s43, 0\n"
    : "+{s[40:41]}"(w0), "+{s[42:43]}"(w1) : "{v108}"(vw0), "{v109}"(vw1), "{s44}"(st), "{s45}"(ring) : "scc", "memory");
}
DI void p1g2_loop(f32x16 (&acc)[4], unsigned long long w0, unsigned long long w1, unsigned vw0, unsigned vw1,
                  const unsigned (&va)[4], unsigned (&xb)[8], unsigned st, unsigned ring, unsigned wrap) {
  asm volatile(
    "v_mov_b32 v0, 0\n"
    "v_mov_b32 v1, 0\n"
    "v_mov_b32 v2, 0\n"
    "v_mov_b32 v3, 0\n"
    "v_mov_b32 v4, 0\n"
    "v_mov_b32 v5, 0\n"
    "v_mov_b32 v6, 0\n"
    "v_mov_b32 v7, 0\n"
    "v_mov_b32 v8, 0\n"
    "v_mov_b32 v9, 0\n"
    "v_mov_b32 v10, 0\n"
    "v_mov_b32 v11, 0\n"
    "v_mov_b32 v12, 0\n"
    "v_mov_b32 v13, 0\n"
    "v_mov_b32 v14, 0\n"
    "v_mov_b32 v15, 0\n"
    "v_mov_b32 v16, 0\n"
    "v_mov_b32 v17, 0\n"
    "v_mov_b32 v18, 0\n"
    "v_mov_b32 v19, 0\n"
    "v_mov_b32 v20, 0\n"
    "v_mov_b32 v21, 0\n"
    "v_mov_b32 v22, 0\n"
    "v_mov_b32 v23, 0\n"
    "v_mov_b32 v24, 0\n"
    "v_mov_b32 v25, 0\n"
    "v_mov_b32 v26, 0\n"
    "v_mov_b32 v27, 0\n"
    "v_mov_b32 v28, 0\n"
    "v_mov_b32 v29, 0\n"
    "v_mov_b32 v30, 0\n"
    "v_mov_b32 v31, 0\n"
    "v_mov_b32 v32, 0\n"
    "v_mov_b32 v33, 0\n"
    "v_mov_b32 v34, 0\n"
    "v_mov_b32 v35, 0\n"
    "v_mov_b32 v36, 0\n"
    "v_mov_b32 v37, 0\n"
    "v_mov_b32 v38, 0\n"
    "v_mov_b32 v39, 0\n"
    "v_mov_b32 v40, 0\n"
    "v_mov_b32 v41, 0\n"
    "v_mov_b32 v42, 0\n"
    "v_mov_b32 v43, 0\n"
    "v_mov_b32 v44, 0\n"
    "v_mov_b32 v45, 0\n"
    "v_mov_b32 v46, 0\n"
    "v_mov_b32 v47, 0\n"
    "v_mov_b32 v48, 0\n"
    "v_mov_b32 v49, 0\n"
    "v_mov_b32 v50, 0\n"
    "v_mov_b32 v51, 0\n"
    "v_mov_b32 v52, 0\n"
    "v_mov_b32 v53, 0\n"
    "v_mov_b32 v54, 0\n"
    "v_mov_b32 v55, 0\n"
    "v_mov_b32 v56, 0\n"
    "v_mov_b32 v57, 0\n"
    "v_mov_b32 v58, 0\n"
    "v_mov_b32 v59, 0\n"
    "v_mov_b32 v60, 0\n"
    "v_mov_b32 v61, 0\n"
    "v_mov_b32 v62, 0\n"
    "v_mov_b32 v63, 0\n"
    "s_setprio 0\n"
    "s_waitcnt vmcnt(6)\n"
    "ds_read_b128 v[72:75], v96 offset:0\n"
    "ds_read_b128 v[76:79], v96 offset:32768\n"
    "ds_read_b64_tr_b16 v[64:65], v104 offset:0\n"
    "ds_read_b64_tr_b16 v[66:67], v105 offset:0\n"
    "ds_read_b64_tr_b16 v[68:69], v104 offset:1024\n"
    "ds_read_b64_tr_b16 v[70:71], v105 offset:1024\n"
    "s_lshl_b32 s54, s44, 5\n"
    "s_mov_b32 s46, 3\n"
    "1:\n"
    "s_cmp_eq_u32 s51, 0\n"
    "s_cselect_b32 s52, s54, 0\n"
    "s_cselect_b32 s53, 0x400, 0\n"
    "s_sub_u32 s53, 0x100, s53\n"
    "s_sub_u32 s51, s51, 1\n"
    "s_waitcnt vmcnt(4) lgkmcnt(0)\n"
    "ds_read_b128 v[88:91], v97 offset:0\n"
    "ds_read_b128 v[92:95], v97 offset:32768\n"
    "ds_read_b64_tr_b16 v[80:81], v104 offset:2048\n"
    "ds_read_b64_tr_b16 v[82:83], v105 offset:2048\n"
    "ds_read_b64_tr_b16 v[84:85], v104 offset:3072\n"
    "ds_read_b64_tr_b16 v[86:87], v105 offset:3072\n"
    "v_mfma_f32_32x32x16_bf16 v[0:15], v[64:67], v[72:75], v[0:15]\n"
    "s_add_u32 m0, s45, 0\n"
    "s_nop 0\n"
    "global_load_lds_dwordx4 v108, s[40:41]\n"
    "s_add_u32 m0, s45, 1024\n"
    "s_nop 0\n"
    "global_load_lds_dwordx4 v108, s[42:43]\n"
    "s_add_u32 s40, s40, s44\n"
    "s_addc_u32 s41, s41, 0\n"
    "s_add_u32 s42, s42, s44\n"
    "s_addc_u32 s43, s43, 0\n"
    "v_mfma_f32_32x32x16_bf16 v[16:31], v[64:67], v[76:79], v[16:31]\n"
    "v_mfma_f32_32x32x16_bf16 v[32:47], v[68:71], v[72:75], v[32:47]\n"
    "v_mfma_f32_32x32x16_bf16 v[48:63], v[68:71], v[76:79], v[48:63]\n"
    "s_waitcnt vmcnt(4) lgkmcnt(0)\n"
    "ds_read_b128 v[72:75], v98 offset:0\n"
    "ds_read_b128 v[76:79], v98 offset:32768\n"
    "ds_read_b64_tr_b16 v[64:65], v104 offset:4096\n"
    "ds_read_b64_tr_b16 v[66:67], v105 offset:4096\n"
    "ds_read_b64_tr_b16 v[68:69], v104 offset:5120\n"
    "ds_read_b64_tr_b16 v[70:71], v105 offset:5120\n"
    "v_mfma_f32_32x32x16_bf16 v[0:15], v[80:83], v[88:91], v[0:15]\n"
    "s_add_u32 m0, s45, 2048\n"
    "s_nop 0\n"
    "global_load_lds_dwordx4 v108, s[40:41]\n"
    "s_add_u32 m0, s45, 3072\n"
    "s_nop 0\n"
    "global_load_lds_dwordx4 v108, s[42:43]\n"
    "s_add_u32 s40, s40, s44\n"
    "s_addc_u32 s41, s41, 0\n"
    "s_add_u32 s42, s42, s44\n"
    "s_addc_u32 s43, s43, 0\n"
    "v_mfma_f32_32x32x16_bf16 v[16:31], v[80:83], v[92:95], v[16:31]\n"
    "v_mfma_f32_32x32x16_bf16 v[32:47], v[84:87], v[88:91], v[32:47]\n"
    "v_mfma_f32_32x32x16_bf16 v[48:63], v[84:87], v[92:95], v[48:63]\n"
    "s_waitcnt vmcnt(4) lgkmcnt(0)\n"
    "ds_read_b128 v[88:91], v99 offset:0\n"
    "ds_read_b128 v[92:95], v99 offset:32768\n"
    "ds_read_b64_tr_b16 v[80:81], v104 offset:6144\n"
    "ds_read_b64_tr_b16 v[82:83], v105 offset:6144\n"
    "ds_read_b64_tr_b16 v[84:85], v104 offset:7168\n"
    "ds_read_b64_tr_b16 v[86:87], v105 offset:7168\n"
    "v_mfma_f32_32x32x16_bf16 v[0:15], v[64:67], v[72:75], v[0:15]\n"
    "s_add_u32 m0, s45, 4096\n"
    "s_nop 0\n"
    "global_load_lds_dwordx4 v108, s[40:41]\n"
    "s_add_u32 m0, s45, 5120\n"
    "s_nop 0\n"
    "global_load_lds_dwordx4 v108, s[42:43]\n"
    "s_add_u32 s40, s40, s44\n"
    "s_addc_u32 s41, s41, 0\n"
    "s_add_u32 s42, s42, s44\n"
    "s_addc_u32 s43, s43, 0\n"
    "v_mfma_f32_32x32x16_bf16 v[16:31], v[64:67], v[76:79], v[16:31]\n"
    "v_mfma_f32_32x32x16_bf16 v[32:47], v[68:71], v[72:75], v[32:47]\n"
    "v_mfma_f32_32x32x16_bf16 v[48:63], v[68:71], v[76:79], v[48:63]\n"
    "s_waitcnt vmcnt(4) lgkmcnt(0)\n"
    "ds_read_b128 v[72:75], v100 offset:0\n"
    "ds_read_b128 v[76:79], v100 offset:32768\n"
    "ds_read_b64_tr_b16 v[64:65], v104 offset:0\n"
    "ds_read_b64_tr_b16 v[66:67], v105 offset:0\n"
    "ds_read_b64_tr_b16 v[68:69], v104 offset:1024\n"
    "ds_read_b64_tr_b16 v[70:71], v105 offset:1024\n"
    "v_mfma_f32_32x32x16_bf16 v[0:15], v[80:83], v[88:91], v[0:15]\n"
    "s_add_u32 m0, s45, 6144\n"
    "s_nop 0\n"
    "global_load_lds_dwordx4 v108, s[40:41]\n"
    "s_add_u32 m0, s45, 7168\n"
    "s_nop 0\n"
    "global_load_lds_dwordx4 v108, s[42:43]\n"
    "s_add_u32 s40, s40, s44\n"
    "s_addc_u32 s41, s41, 0\n"
    "s_add_u32 s42, s42, s44\n"
    "s_addc_u32 s43, s43, 0\n"
    "v_mfma_f32_32x32x16_bf16 v[16:31], v[80:83], v[92:95], v[16:31]\n"
    "v_mfma_f32_32x32x16_bf16 v[32:47], v[84:87], v[88:91], v[32:47]\n"
    "v_mfma_f32_32x32x16_bf16 v[48:63], v[84:87], v[92:95], v[48:63]\n"
    "s_sub_u32 s40, s40, s52\n"
    "s_subb_u32 s41, s41, 0\n"
    "s_sub_u32 s42, s42, s52\n"
    "s_subb_u32 s43, s43, 0\n"
    "s_waitcnt vmcnt(4) lgkmcnt(0)\n"
    "ds_read_b128 v[88:91], v101 offset:0\n"
    "ds_read_b128 v[92:95], v101 offset:32768\n"
    "ds_read_b64_tr_b16 v[80:81], v104 offset:2048\n"
    "ds_read_b64_tr_b16 v[82:83], v105 offset:2048\n"
    "ds_read_b64_tr_b16 v[84:85], v104 offset:3072\n"
    "ds_read_b64_tr_b16 v[86:87], v105 offset:3072\n"
    "v_mfma_f32_32x32x16_bf16 v[0:15], v[64:67], v[72:75], v[0:15]\n"
    "s_add_u32 m0, s45, 0\n"
    "s_nop 0\n"
    "global_load_lds_dwordx4 v108, s[40:41]\n"
    "s_add_u32 m0, s45, 1024\n"
    "s_nop 0\n"
    "global_load_lds_dwordx4 v108, s[42:43]\n"
    "s_add_u32 s40, s40, s44\n"
    "s_addc_u32 s41, s41, 0\n"
    "s_add_u32 s42, s42, s44\n"
    "s_addc_u32 s43, s43, 0\n"
    "v_mfma_f32_32x32x16_bf16 v[16:31], v[64:67], v[76:79], v[16:31]\n"
    "v_mfma_f32_32x32x16_bf16 v[32:47], v[68:71], v[72:75], v[32:47]\n"
    "v_mfma_f32_32x32x16_bf16 v[48:63], v[68:71], v[76:79], v[48:63]\n"
    "s_waitcnt vmcnt(4) lgkmcnt(0)\n"
    "ds_read_b128 v[72:75], v102 offset:0\n"
    "ds_read_b128 v[76:79], v102 offset:32768\n"
    "ds_read_b64_tr_b16 v[64:65], v104 offset:4096\n"
    "ds_read_b64_tr_b16 v[66:67], v105 offset:4096\n"
    "ds_read_b64_tr_b16 v[68:69], v104 offset:5120\n"
    "ds_read_b64_tr_b16 v[70:71], v105 offset:5120\n"
    "v_mfma_f32_32x32x16_bf16 v[0:15], v[80:83], v[88:91], v[0:15]\n"
    "s_add_u32 m0, s45, 2048\n"
    "s_nop 0\n"
    "global_load_lds_dwordx4 v108, s[40:41]\n"
    "s_add_u32 m0, s45, 3072\n"
    "s_nop 0\n"
    "global_load_lds_dwordx4 v108, s[42:43]\n"
    "s_add_u32 s40, s40, s44\n"
    "s_addc_u32 s41, s41, 0\n"
    "s_add_u32 s42, s42, s44\n"
    "s_addc_u32 s43, s43, 0\n"
    "v_mfma_f32_32x32x16_bf16 v[16:31], v[80:83], v[92:95], v[16:31]\n"
    "v_mfma_f32_32x32x16_bf16 v[32:47], v[84:87], v[88:91], v[32:47]\n"
    "v_mfma_f32_32x32x16_bf16 v[48:63], v[84:87], v[92:95], v[48:63]\n"
    "s_waitcnt vmcnt(4) lgkmcnt(0)\n"
    "ds_read_b128 v[88:91], v103 offset:0\n"
    "ds_read_b128 v[92:95], v103 offset:32768\n"
    "ds_read_b64_tr_b16 v[80:81], v104 offset:6144\n"
    "ds_read_b64_tr_b16 v[82:83], v105 offset:6144\n"
    "ds_read_b64_tr_b16 v[84:85], v104 offset:7168\n"
    "ds_read_b64_tr_b16 v[86:87], v105 offset:7168\n"
    "v_mfma_f32_32x32x16_bf16 v[0:15], v[64:67], v[72:75], v[0:15]\n"
    "s_add_u32 m0, s45, 4096\n"
    "s_nop 0\n"
    "global_load_lds_dwordx4 v108, s[40:41]\n"
    "s_add_u32 m0, s45, 5120\n"
    "s_nop 0\n"
    "global_load_lds_dwordx4 v108, s[42:43]\n"
    "s_add_u32 s40, s40, s44\n"
    "s_addc_u32 s41, s41, 0\n"
    "s_add_u32 s42, s42, s44\n"
    "s_addc_u32 s43, s43, 0\n"
    "v_mfma_f32_32x32x16_bf16 v[16:31], v[64:67], v[76:79], v[16:31]\n"
    "v_mfma_f32_32x32x16_bf16 v[32:47], v[68:71], v[72:75], v[32:47]\n"
    "v_mfma_f32_32x32x16_bf16 v[48:63], v[68:71], v[76:79], v[48:63]\n"
    "s_waitcnt vmcnt(4) lgkmcnt(0)\n"
    "s_setprio 3\n"
    "v_add_u32 v96, s53, v96\n"
    "v_add_u32 v97, s53, v97\n"
    "v_add_u32 v98, s53, v98\n"
    "v_add_u32 v99, s53, v99\n"
    "v_add_u32 v100, s53, v100\n"
    "v_add_u32 v101, s53, v101\n"
    "v_add_u32 v102, s53, v102\n"
    "v_add_u32 v103, s53, v103\n"
    "s_setprio 0\n"
    "ds_read_b128 v[72:75], v96 offset:0\n"
    "ds_read_b128 v[76:79], v96 offset:32768\n"
    "ds_read_b64_tr_b16 v[64:65], v104 offset:0\n"
    "ds_read_b64_tr_b16 v[66:67], v105 offset:0\n"
    "ds_read_b64_tr_b16 v[68:69], v104 offset:1024\n"
    "ds_read_b64_tr_b16 v[70:71], v105 offset:1024\n"
    "v_mfma_f32_32x32x16_bf16 v[0:15], v[80:83], v[88:91], v[0:15]\n"
    "s_add_u32 m0, s45, 6144\n"
    "s_nop 0\n"
    "global_load_lds_dwordx4 v108, s[40:41]\n"
    "s_add_u32 m0, s45, 7168\n"
    "s_nop 0\n"
    "global_load_lds_dwordx4 v108, s[42:43]\n"
    "s_add_u32 s40, s40, s44\n"
    "s_addc_u32 s41, s41, 0\n"
    "s_add_u32 s42, s42, s44\n"
    "s_addc_u32 s43, s43, 0\n"
    "v_mfma_f32_32x32x16_bf16 v[16:31], v[80:83], v[92:95], v[16:31]\n"
    "v_mfma_f32_32x32x16_bf16 v[32:47], v[84:87], v[88:91], v[32:47]\n"
    "v_mfma_f32_32x32x16_bf16 v[48:63], v[84:87], v[92:95], v[48:63]\n"
    "s_sub_u32 s46, s46, 1\n"
    "s_cmp_lg_u32 s46, 0\n"
    "s_cbranch_scc1 1b\n"
    "s_waitcnt vmcnt(4) lgkmcnt(0)\n"
    "ds_read_b128 v[88:91], v97 offset:0\n"
    "ds_read_b128 v[92:95], v97 offset:32768\n"
    "ds_read_b64_tr_b16 v[80:81], v104 offset:2048\n"
    "ds_read_b64_tr_b16 v[82:83], v105 offset:2048\n"
    "ds_read_b64_tr_b16 v[84:85], v104 offset:3072\n"
    "ds_read_b64_tr_b16 v[86:87], v105 offset:3072\n"
    "v_mfma_f32_32x32x16_bf16 v[0:15], v[64:67], v[72:75], v[0:15]\n"
    "s_add_u32 m0, s45, 0\n"
    "s_nop 0\n"
    "global_load_lds_dwordx4 v108, s[40:41]\n"
    "s_add_u32 m0, s45, 1024\n"
    "s_nop 0\n"
    "global_load_lds_dwordx4 v108, s[42:43]\n"
    "s_add_u32 s40, s40, s44\n"
    "s_addc_u32 s41, s41, 0\n"
    "s_add_u32 s42, s42, s44\n"
    "s_addc_u32 s43, s43, 0\n"
    "v_mfma_f32_32x32x16_bf16 v[16:31], v[64:67], v[76:79], v[16:31]\n"
    "v_mfma_f32_32x32x16_bf16 v[32:47], v[68:71], v[72:75], v[32:47]\n"
    "v_mfma_f32_32x32x16_bf16 v[48:63], v[68:71], v[76:79], v[48:63]\n"
    "s_waitcnt vmcnt(4) lgkmcnt(0)\n"
    "ds_read_b128 v[72:75], v98 offset:0\n"
    "ds_read_b128 v[76:79], v98 offset:32768\n"
    "ds_read_b64_tr_b16 v[64:65], v104 offset:4096\n"
    "ds_read_b64_tr_b16 v[66:67], v105 offset:4096\n"
    "ds_read_b64_tr_b16 v[68:69], v104 offset:5120\n"
    "ds_read_b64_tr_b16 v[70:71], v105 offset:5120\n"
    "v_mfma_f32_32x32x16_bf16 v[0:15], v[80:83], v[88:91], v[0:15]\n"
    "s_add_u32 m0, s45, 2048\n"
    "s_nop 0\n"
    "global_load_lds_dwordx4 v108, s[40:41]\n"
    "s_add_u32 m0, s45, 3072\n"
    "s_nop 0\n"
    "global_load_lds_dwordx4 v108, s[42:43]\n"
    "s_add_u32 s40, s40, s44\n"
    "s_addc_u32 s41, s41, 0\n"
    "s_add_u32 s42, s42, s44\n"
    "s_addc_u32 s43, s43, 0\n"
    "v_mfma_f32_32x32x16_bf16 v[16:31], v[80:83], v[92:95], v[16:31]\n"
    "v_mfma_f32_32x32x16_bf16 v[32:47], v[84:87], v[88:91], v[32:47]\n"
    "v_mfma_f32_32x32x16_bf16 v[48:63], v[84:87], v[92:95], v[48:63]\n"
    "s_waitcnt vmcnt(4) lgkmcnt(0)\n"
    "ds_read_b128 v[88:91], v99 offset:0\n"
    "ds_read_b128 v[92:95], v99 offset:32768\n"
    "ds_read_b64_tr_b16 v[80:81], v104 offset:6144\n"
    "ds_read_b64_tr_b16 v[82:83], v105 offset:6144\n"
    "ds_read_b64_tr_b16 v[84:85], v104 offset:7168\n"
    "ds_read_b64_tr_b16 v[86:87], v105 offset:7168\n"
    "v_mfma_f32_32x32x16_bf16 v[0:15], v[64:67], v[72:75], v[0:15]\n"
    "s_add_u32 m0, s45, 4096\n"
    "s_nop 0\n"
    "global_load_lds_dwordx4 v108, s[40:41]\n"
    "s_add_u32 m0, s45, 5120\n"
    "s_nop 0\n"
    "global_load_lds_dwordx4 v108, s[42:43]\n"
    "s_add_u32 s40, s40, s44\n"
    "s_addc_u32 s41, s41, 0\n"
    "s_add_u32 s42, s42, s44\n"
    "s_addc_u32 s43, s43, 0\n"
    "v_mfma_f32_32x32x16_bf16 v[16:31], v[64:67], v[76:79], v[16:31]\n"
    "v_mfma_f32_32x32x16_bf16 v[32:47], v[68:71], v[72:75], v[32:47]\n"
    "v_mfma_f32_32x32x16_bf16 v[48:63], v[68:71], v[76:79], v[48:63]\n"
    "s_waitcnt vmcnt(4) lgkmcnt(0)\n"
    "ds_read_b128 v[72:75], v100 offset:0\n"
    "ds_read_b128 v[76:79], v100 offset:32768\n"
    "ds_read_b64_tr_b16 v[64:65], v104 offset:0\n"
    "ds_read_b64_tr_b16 v[66:67], v105 offset:0\n"
    "ds_read_b64_tr_b16 v[68:69], v104 offset:1024\n"
    "ds_read_b64_tr_b16 v[70:71], v105 offset:1024\n"
    "v_mfma_f32_32x32x16_bf16 v[0:15], v[80:83], v[88:91], v[0:15]\n"
    "s_add_u32 m0, s45, 6144\n"
    "s_nop 0\n"
    "global_load_lds_dwordx4 v108, s[40:41]\n"
    "s_add_u32 m0, s45, 7168\n"
    "s_nop 0\n"
    "global_load_lds_dwordx4 v108, s[42:43]\n"
    "s_add_u32 s40, s40, s44\n"
    "s_addc_u32 s41, s41, 0\n"
    "s_add_u32 s42, s42, s44\n"
    "s_addc_u32 s43, s43, 0\n"
    "v_mfma_f32_32x32x16_bf16 v[16:31], v[80:83], v[92:95], v[16:31]\n"
    "v_mfma_f32_32x32x16_bf16 v[32:47], v[84:87], v[88:91], v[32:47]\n"
    "v_mfma_f32_32x32x16_bf16 v[48:63], v[84:87], v[92:95], v[48:63]\n"
    "s_waitcnt vmcnt(4) lgkmcnt(0)\n"
    "ds_read_b128 v[88:91], v101 offset:0\n"
    "ds_read_b128 v[92:95], v101 offset:32768\n"
    "ds_read_b64_tr_b16 v[80:81], v104 offset:2048\n"
    "ds_read_b64_tr_b16 v[82:83], v105 offset:2048\n"
    "ds_read_b64_tr_b16 v[84:85], v104 offset:3072\n"
    "ds_read_b64_tr_b16 v[86:87], v105 offset:3072\n"
    "v_mfma_f32_32x32x16_bf16 v[0:15], v[64:67], v[72:75], v[0:15]\n"
    "v_mfma_f32_32x32x16_bf16 v[16:31], v[64:67], v[76:79], v[16:31]\n"
    "v_mfma_f32_32x32x16_bf16 v[32:47], v[68:71], v[72:75], v[32:47]\n"
    "v_mfma_f32_32x32x16_bf16 v[48:63], v[68:71], v[76:79], v[48:63]\n"
    "s_waitcnt vmcnt(2) lgkmcnt(0)\n"
    "ds_read_b128 v[72:75], v102 offset:0\n"
    "ds_read_b128 v[76:79], v102 offset:32768\n"
    "ds_read_b64_tr_b16 v[64:65], v104 offset:4096\n"
    "ds_read_b64_tr_b16 v[66:67], v105 offset:4096\n"
    "ds_read_b64_tr_b16 v[68:69], v104 offset:5120\n"
    "ds_read_b64_tr_b16 v[70:71], v105 offset:5120\n"
    "v_mfma_f32_32x32x16_bf16 v[0:15], v[80:83], v[88:91], v[0:15]\n"
    "v_mfma_f32_32x32x16_bf16 v[16:31], v[80:83], v[92:95], v[16:31]\n"
    "v_mfma_f32_32x32x16_bf16 v[32:47], v[84:87], v[88:91], v[32:47]\n"
    "v_mfma_f32_32x32x16_bf16 v[48:63], v[84:87], v[92:95], v[48:63]\n"
    "s_waitcnt vmcnt(0) lgkmcnt(0)\n"
    "ds_read_b128 v[88:91], v103 offset:0\n"
    "ds_read_b128 v[92:95], v103 offset:32768\n"
    "ds_read_b64_tr_b16 v[80:81], v104 offset:6144\n"
    "ds_read_b64_tr_b16 v[82:83], v105 offset:6144\n"
    "ds_read_b64_tr_b16 v[84:85], v104 offset:7168\n"
    "ds_read_b64_tr_b16 v[86:87], v105 offset:7168\n"
    "v_mfma_f32_32x32x16_bf16 v[0:15], v[64:67], v[72:75], v[0:15]\n"
    "v_mfma_f32_32x32x16_bf16 v[16:31], v[64:67], v[76:79], v[16:31]\n"
    "v_mfma_f32_32x32x16_bf16 v[32:47], v[68:71], v[72:75], v[32:47]\n"
    "v_mfma_f32_32x32x16_bf16 v[48:63], v[68:71], v[76:79], v[48:63]\n"
    "s_waitcnt lgkmcnt(0)\n"
    "v_mfma_f32_32x32x16_bf16 v[0:15], v[80:83], v[88:91], v[0:15]\n"
    "v_mfma_f32_32x32x16_bf16 v[16:31], v[80:83], v[92:95], v[16:31]\n"
    "v_mfma_f32_32x32x16_bf16 v[32:47], v[84:87], v[88:91], v[32:47]\n"
    "v_mfma_f32_32x32x16_bf16 v[48:63], v[84:87], v[92:95], v[48:63]\n"
    "s_setprio 2\n"
    "s_nop 7\n"
    "s_nop 7\n"
    : "={v[0:15]}"(acc[0]), "={v[16:31]}"(acc[1]), "={v[32:47]}"(acc[2]), "={v[48:63]}"(acc[3]), "+{s[40:41]}"(w0), "+{s[42:43]}"(w1), "+{v96}"(xb[0]), "+{v97}"(xb[1]), "+{v98}"(xb[2]), "+{v99}"(xb[3]), "+{v100}"(xb[4]), "+{v101}"(xb[5]), "+{v102}"(xb[6]), "+{v103}"(xb[7]), "+{s51}"(wrap)
    : "{v104}"(va[0]), "{v105}"(va[1]), "{v106}"(va[2]), "{v107}"(va[3]), "{v108}"(vw0), "{v109}"(vw1), "{s44}"(st), "{s45}"(ring)
    : "v64", "v65", "v66", "v67", "v68", "v69", "v70", "v71", "v72", "v73", "v74", "v75", "v76", "v77", "v78", "v79", "v80", "v81", "v82", "v83", "v84", "v85", "v86", "v87", "v88", "v89", "v90", "v91", "v92", "v93", "v94", "v95", "s46", "s52", "s53", "s54", "scc", "memory");
}


DI void p2_prefill(unsigned long long& w, unsigned vw0, unsigned vw1, unsigned ring, unsigned& wrap) {
  asm volatile(
    "s_add_u32 m0, s45, 0\n"
    "s_nop 0\n"
    "global_load_lds_dwordx4 v66, s[40:41]\n"
    "s_add_u32 m0, s45, 1024\n"
    "s_nop 0\n"
    "global_load_lds_dwordx4 v67, s[40:41]\n"
    "s_add_u32 s40, s40, 0x4000\n"
    "s_addc_u32 s41, s41, 0\n"
    "s_add_u32 m0, s45, 2048\n"
    "s_nop 0\n"
    "global_load_lds_dwordx4 v66, s[40:41]\n"
    "s_add_u32 m0, s45, 3072\n"
    "s_nop 0\n"
    "global_load_lds_dwordx4 v67, s[40:41]\n"
    "s_add_u32 s40, s40, 0x4000\n"
    "s_addc_u32 s41, s41, 0\n"
    "s_add_u32 m0, s45, 4096\n"
    "s_nop 0\n"
    "global_load_lds_dwordx4 v66, s[40:41]\n"
    "s_add_u32 m0, s45, 5120\n"
    "s_nop 0\n"
    "global_load_lds_dwordx4 v67, s[40:41]\n"
    "s_add_u32 s40, s40, 0x4000\n"
    "s_addc_u32 s41, s41, 0\n"
    "s_add_u32 m0, s45, 6144\n"
    "s_nop 0\n"
    "global_load_lds_dwordx4 v66, s[40:41]\n"
    "s_add_u32 m0, s45, 7168\n"
    "s_nop 0\n"
    "global_load_lds_dwordx4 v67, s[40:41]\n"
    "s_add_u32 s40, s40, 0x4000\n"
    "s_addc_u32 s41, s41, 0\n"
    "s_sub_u32 s51, s51, 1\n"
    "s_cmp_eq_u32 s51, 0\n"
    "s_cselect_b32 s52, 458752, 0\n"
    "s_sub_u32 s40, s40, s52\n"
    "s_subb_u32 s41, s41, 0\n"
    "s_add_u32 m0, s45, 8192\n"
    "s_nop 0\n"
    "global_load_lds_dwordx4 v66, s[40:41]\n"
    "s_add_u32 m0, s45, 9216\n"
    "s_nop 0\n"
    "global_load_lds_dwordx4 v67, s[40:41]\n"
    "s_add_u32 s40, s40, 0x4000\n"
    "s_addc_u32 s41, s41, 0\n"
    "s_add_u32 m0, s45, 10240\n"
    "s_nop 0\n"
    "global_load_lds_dwordx4 v66, s[40:41]\n"
    "s_add_u32 m0, s45, 11264\n"
    "s_nop 0\n"
    "global_load_lds_dwordx4 v67, s[40:41]\n"
    "s_add_u32 s40, s40, 0x4000\n"
    "s_addc_u32 s41, s41, 0\n"
    "s_add_u32 m0, s45, 12288\n"
    "s_nop 0\n"
    "global_load_lds_dwordx4 v66, s[40:41]\n"
    "s_add_u32 m0, s45, 13312\n"
    "s_nop 0\n"
    "global_load_lds_dwordx4 v67, s[40:41]\n"
    "s_add_u32 s40, s40, 0x4000\n"
    "s_addc_u32 s41, s41, 0\n"
    "s_add_u32 m0, s45, 14336\n"
    "s_nop 0\n"
    "global_load_lds_dwordx4 v66, s[40:41]\n"
    "s_add_u32 m0, s45, 15360\n"
    "s_nop 0\n"
    "global_load_lds_dwordx4 v67, s[40:41]\n"
    "s_add_u32 s40, s40, 0x4000\n"
    "s_addc_u32 s41, s41, 0\n"
    : "+{s[40:41]}"(w), "+{s51}"(wrap) : "{v66}"(vw0), "{v67}"(vw1), "{s45}"(ring) : "s52", "scc", "memory");
}
DI void p2_run(f32x16& a00, f32x16& a01, f32x16& a10, f32x16& a11, unsigned long long w, unsigned long long b0, unsigned long long b1,
               unsigned va0, unsigned va1, unsigned vw0, unsigned vw1, unsigned voff, unsigned ring, unsigned wrapw, unsigned wrapb) {
  asm volatile(
    "global_load_dwordx4 v[88:91], v68, s[46:47] sc1\n"
    "global_load_dwordx4 v[92:95], v68, s[48:49] sc1\n"
    "s_add_u32 s46, s46, 0x400\n"
    "s_addc_u32 s47, s47, 0\n"
    "s_add_u32 s48, s48, 0x400\n"
    "s_addc_u32 s49, s49, 0\n"
    "global_load_dwordx4 v[96:99], v68, s[46:47] sc1\n"
    "global_load_dwordx4 v[100:103], v68, s[48:49] sc1\n"
    "s_add_u32 s46, s46, 0x400\n"
    "s_addc_u32 s47, s47, 0\n"
    "s_add_u32 s48, s48, 0x400\n"
    "s_addc_u32 s49, s49, 0\n"
    "global_load_dwordx4 v[104:107], v68, s[46:47] sc1\n"
    "global_load_dwordx4 v[108:111], v68, s[48:49] sc1\n"
    "s_add_u32 s46, s46, 0x400\n"
    "s_addc_u32 s47, s47, 0\n"
    "s_add_u32 s48, s48, 0x400\n"
    "s_addc_u32 s49, s49, 0\n"
    "global_load_dwordx4 v[112:115], v68, s[46:47] sc1\n"
    "global_load_dwordx4 v[116:119], v68, s[48:49] sc1\n"
    "s_add_u32 s46, s46, 0x400\n"
    "s_addc_u32 s47, s47, 0\n"
    "s_add_u32 s48, s48, 0x400\n"
    "s_addc_u32 s49, s49, 0\n"
    "s_sub_u32 s55, s55, 1\n"
    "s_cmp_eq_u32 s55, 0\n"
    "s_cselect_b32 s53, 28672, 0\n"
    "s_sub_u32 s46, s46, s53\n"
    "s_subb_u32 s47, s47, 0\n"
    "s_sub_u32 s48, s48, s53\n"
    "s_subb_u32 s49, s49, 0\n"
    "global_load_dwordx4 v[120:123], v68, s[46:47] sc1\n"
    "global_load_dwordx4 v[124:127], v68, s[48:49] sc1\n"
    "s_add_u32 s46, s46, 0x400\n"
    "s_addc_u32 s47, s47, 0\n"
    "s_add_u32 s48, s48, 0x400\n"
    "s_addc_u32 s49, s49, 0\n"
    "global_load_dwordx4 v[128:131], v68, s[46:47] sc1\n"
    "global_load_dwordx4 v[132:135], v68, s[48:49] sc1\n"
    "s_add_u32 s46, s46, 0x400\n"
    "s_addc_u32 s47, s47, 0\n"
    "s_add_u32 s48, s48, 0x400\n"
    "s_addc_u32 s49, s49, 0\n"
    "global_load_dwordx4 v[136:139], v68, s[46:47] sc1\n"
    "global_load_dwordx4 v[140:143], v68, s[48:49] sc1\n"
    "s_add_u32 s46, s46, 0x400\n"
    "s_addc_u32 s47, s47, 0\n"
    "s_add_u32 s48, s48, 0x400\n"
    "s_addc_u32 s49, s49, 0\n"
    "global_load_dwordx4 v[144:147], v68, s[46:47] sc1\n"
    "global_load_dwordx4 v[148:151], v68, s[48:49] sc1\n"
    "s_add_u32 s46, s46, 0x400\n"
    "s_addc_u32 s47, s47, 0\n"
    "s_add_u32 s48, s48, 0x400\n"
    "s_addc_u32 s49, s49, 0\n"
    "v_mov_b32 v0, 0\n"
    "v_mov_b32 v1, 0\n"
    "v_mov_b32 v2, 0\n"
    "v_mov_b32 v3, 0\n"
    "v_mov_b32 v4, 0\n"
    "v_mov_b32 v5, 0\n"
    "v_mov_b32 v6, 0\n"
    "v_mov_b32 v7, 0\n"
    "v_mov_b32 v8, 0\n"
    "v_mov_b32 v9, 0\n"
    "v_mov_b32 v10, 0\n"
    "v_mov_b32 v11, 0\n"
    "v_mov_b32 v12, 0\n"
    "v_mov_b32 v13, 0\n"
    "v_mov_b32 v14, 0\n"
    "v_mov_b32 v15, 0\n"
    "v_mov_b32 v16, 0\n"
    "v_mov_b32 v17, 0\n"
    "v_mov_b32 v18, 0\n"
    "v_mov_b32 v19, 0\n"
    "v_mov_b32 v20, 0\n"
    "v_mov_b32 v21, 0\n"
    "v_mov_b32 v22, 0\n"
    "v_mov_b32 v23, 0\n"
    "v_mov_b32 v24, 0\n"
    "v_mov_b32 v25, 0\n"
    "v_mov_b32 v26, 0\n"
    "v_mov_b32 v27, 0\n"
    "v_mov_b32 v28, 0\n"
    "v_mov_b32 v29, 0\n"
    "v_mov_b32 v30, 0\n"
    "v_mov_b32 v31, 0\n"
    "v_mov_b32 v32, 0\n"
    "v_mov_b32 v33, 0\n"
    "v_mov_b32 v34, 0\n"
    "v_mov_b32 v35, 0\n"
    "v_mov_b32 v36, 0\n"
    "v_mov_b32 v37, 0\n"
    "v_mov_b32 v38, 0\n"
    "v_mov_b32 v39, 0\n"
    "v_mov_b32 v40, 0\n"
    "v_mov_b32 v41, 0\n"
    "v_mov_b32 v42, 0\n"
    "v_mov_b32 v43, 0\n"
    "v_mov_b32 v44, 0\n"
    "v_mov_b32 v45, 0\n"
    "v_mov_b32 v46, 0\n"
    "v_mov_b32 v47, 0\n"
    "v_mov_b32 v48, 0\n"
    "v_mov_b32 v49, 0\n"
    "v_mov_b32 v50, 0\n"
    "v_mov_b32 v51, 0\n"
    "v_mov_b32 v52, 0\n"
    "v_mov_b32 v53, 0\n"
    "v_mov_b32 v54, 0\n"
    "v_mov_b32 v55, 0\n"
    "v_mov_b32 v56, 0\n"
    "v_mov_b32 v57, 0\n"
    "v_mov_b32 v58, 0\n"
    "v_mov_b32 v59, 0\n"
    "v_mov_b32 v60, 0\n"
    "v_mov_b32 v61, 0\n"
    "v_mov_b32 v62, 0\n"
    "v_mov_b32 v63, 0\n"
    "s_setprio 0\n"
    "s_waitcnt vmcnt(16)\n"
    "ds_read_b64_tr_b16 v[72:73], v64 offset:0\n"
    "ds_read_b64_tr_b16 v[74:75], v64 offset:1024\n"
    "ds_read_b64_tr_b16 v[76:77], v65 offset:0\n"
    "ds_read_b64_tr_b16 v[78:79], v65 offset:1024\n"
    "s_waitcnt vmcnt(14) lgkmcnt(0)\n"
    "ds_read_b64_tr_b16 v[80:81], v64 offset:2048\n"
    "ds_read_b64_tr_b16 v[82:83], v64 offset:3072\n"
    "ds_read_b64_tr_b16 v[84:85], v65 offset:2048\n"
    "ds_read_b64_tr_b16 v[86:87], v65 offset:3072\n"
    "v_mfma_f32_32x32x16_bf16 v[0:15], v[72:75], v[88:91], v[0:15]\n"
    "v_mfma_f32_32x32x16_bf16 v[16:31], v[72:75], v[92:95], v[16:31]\n"
    "v_mfma_f32_32x32x16_bf16 v[32:47], v[76:79], v[88:91], v[32:47]\n"
    "v_mfma_f32_32x32x16_bf16 v[48:63], v[76:79], v[92:95], v[48:63]\n"
    "s_sub_u32 s51, s51, 1\n"
    "s_cmp_eq_u32 s51, 0\n"
    "s_cselect_b32 s52, 458752, 0\n"
    "s_sub_u32 s40, s40, s52\n"
    "s_subb_u32 s41, s41, 0\n"
    "s_add_u32 m0, s45, 0\n"
    "s_nop 0\n"
    "global_load_lds_dwordx4 v66, s[40:41]\n"
    "s_add_u32 m0, s45, 1024\n"
    "s_nop 0\n"
    "global_load_lds_dwordx4 v67, s[40:41]\n"
    "s_add_u32 s40, s40, 0x4000\n"
    "s_addc_u32 s41, s41, 0\n"
    "s_sub_u32 s55, s55, 1\n"
    "s_cmp_eq_u32 s55, 0\n"
    "s_cselect_b32 s53, 28672, 0\n"
    "s_sub_u32 s46, s46, s53\n"
    "s_subb_u32 s47, s47, 0\n"
    "s_sub_u32 s48, s48, s53\n"
    "s_subb_u32 s49, s49, 0\n"
    "global_load_dwordx4 v[88:91], v68, s[46:47] sc1\n"
    "global_load_dwordx4 v[92:95], v68, s[48:49] sc1\n"
    "s_add_u32 s46, s46, 0x400\n"
    "s_addc_u32 s47, s47, 0\n"
    "s_add_u32 s48, s48, 0x400\n"
    "s_addc_u32 s49, s49, 0\n"
    "s_waitcnt vmcnt(16) lgkmcnt(0)\n"
    "ds_read_b64_tr_b16 v[72:73], v64 offset:4096\n"
    "ds_read_b64_tr_b16 v[74:75], v64 offset:5120\n"
    "ds_read_b64_tr_b16 v[76:77], v65 offset:4096\n"
    "ds_read_b64_tr_b16 v[78:79], v65 offset:5120\n"
    "v_mfma_f32_32x32x16_bf16 v[0:15], v[80:83], v[96:99], v[0:15]\n"
    "v_mfma_f32_32x32x16_bf16 v[16:31], v[80:83], v[100:103], v[16:31]\n"
    "v_mfma_f32_32x32x16_bf16 v[32:47], v[84:87], v[96:99], v[32:47]\n"
    "v_mfma_f32_32x32x16_bf16 v[48:63], v[84:87], v[100:103], v[48:63]\n"
    "s_add_u32 m0, s45, 2048\n"
    "s_nop 0\n"
    "global_load_lds_dwordx4 v66, s[40:41]\n"
    "s_add_u32 m0, s45, 3072\n"
    "s_nop 0\n"
    "global_load_lds_dwordx4 v67, s[40:41]\n"
    "s_add_u32 s40, s40, 0x4000\n"
    "s_addc_u32 s41, s41, 0\n"
    "global_load_dwordx4 v[96:99], v68, s[46:47] sc1\n"
    "global_load_dwordx4 v[100:103], v68, s[48:49] sc1\n"
    "s_add_u32 s46, s46, 0x400\n"
    "s_addc_u32 s47, s47, 0\n"
    "s_add_u32 s48, s48, 0x400\n"
    "s_addc_u32 s49, s49, 0\n"
    "s_waitcnt vmcnt(18) lgkmcnt(0)\n"
    "ds_read_b64_tr_b16 v[80:81], v64 offset:6144\n"
    "ds_read_b64_tr_b16 v[82:83], v64 offset:7168\n"
    "ds_read_b64_tr_b16 v[84:85], v65 offset:6144\n"
    "ds_read_b64_tr_b16 v[86:87], v65 offset:7168\n"
    "v_mfma_f32_32x32x16_bf16 v[0:15], v[72:75], v[104:107], v[0:15]\n"
    "v_mfma_f32_32x32x16_bf16 v[16:31], v[72:75], v[108:111], v[16:31]\n"
    "v_mfma_f32_32x32x16_bf16 v[32:47], v[76:79], v[104:107], v[32:47]\n"
    "v_mfma_f32_32x32x16_bf16 v[48:63], v[76:79], v[108:111], v[48:63]\n"
    "s_add_u32 m0, s45, 4096\n"
    "s_nop 0\n"
    "global_load_lds_dwordx4 v66, s[40:41]\n"
    "s_add_u32 m0, s45, 5120\n"
    "s_nop 0\n"
    "global_load_lds_dwordx4 v67, s[40:41]\n"
    "s_add_u32 s40, s40, 0x4000\n"
    "s_addc_u32 s41, s41, 0\n"
    "global_load_dwordx4 v[104:107], v68, s[46:47] sc1\n"
    "global_load_dwordx4 v[108:111], v68, s[48:49] sc1\n"
    "s_add_u32 s46, s46, 0x400\n"
    "s_addc_u32 s47, s47, 0\n"
    "s_add_u32 s48, s48, 0x400\n"
    "s_addc_u32 s49, s49, 0\n"
    "s_waitcnt vmcnt(20) lgkmcnt(0)\n"
    "ds_read_b64_tr_b16 v[72:73], v64 offset:8192\n"
    "ds_read_b64_tr_b16 v[74:75], v64 offset:9216\n"
    "ds_read_b64_tr_b16 v[76:77], v65 offset:8192\n"
    "ds_read_b64_tr_b16 v[78:79], v65 offset:9216\n"
    "v_mfma_f32_32x32x16_bf16 v[0:15], v[80:83], v[112:115], v[0:15]\n"
    "v_mfma_f32_32x32x16_bf16 v[16:31], v[80:83], v[116:119], v[16:31]\n"
    "v_mfma_f32_32x32x16_bf16 v[32:47], v[84:87], v[112:115], v[32:47]\n"
    "v_mfma_f32_32x32x16_bf16 v[48:63], v[84:87], v[116:119], v[48:63]\n"
    "s_add_u32 m0, s45, 6144\n"
    "s_nop 0\n"
    "global_load_lds_dwordx4 v66, s[40:41]\n"
    "s_add_u32 m0, s45, 7168\n"
    "s_nop 0\n"
    "global_load_lds_dwordx4 v67, s[40:41]\n"
    "s_add_u32 s40, s40, 0x4000\n"
    "s_addc_u32 s41, s41, 0\n"
    "global_load_dwordx4 v[112:115], v68, s[46:47] sc1\n"
    "global_load_dwordx4 v[116:119], v68, s[48:49] sc1\n"
    "s_add_u32 s46, s46, 0x400\n"
    "s_addc_u32 s47, s47, 0\n"
    "s_add_u32 s48, s48, 0x400\n"
    "s_addc_u32 s49, s49, 0\n"
    "s_waitcnt vmcnt(22) lgkmcnt(0)\n"
    "ds_read_b64_tr_b16 v[80:81], v64 offset:10240\n"
    "ds_read_b64_tr_b16 v[82:83], v64 offset:11264\n"
    "ds_read_b64_tr_b16 v[84:85], v65 offset:10240\n"
    "ds_read_b64_tr_b16 v[86:87], v65 offset:11264\n"
    "v_mfma_f32_32x32x16_bf16 v[0:15], v[72:75], v[120:123], v[0:15]\n"
    "v_mfma_f32_32x32x16_bf16 v[16:31], v[72:75], v[124:127], v[16:31]\n"
    "v_mfma_f32_32x32x16_bf16 v[32:47], v[76:79], v[120:123], v[32:47]\n"
    "v_mfma_f32_32x32x16_bf16 v[48:63], v[76:79], v[124:127], v[48:63]\n"
    "s_sub_u32 s51, s51, 1\n"
    "s_cmp_eq_u32 s51, 0\n"
    "s_cselect_b32 s52, 458752, 0\n"
    "s_sub_u32 s40, s40, s52\n"
    "s_subb_u32 s41, s41, 0\n"
    "s_add_u32 m0, s45, 8192\n"
    "s_nop 0\n"
    "global_load_lds_dwordx4 v66, s[40:41]\n"
    "s_add_u32 m0, s45, 9216\n"
    "s_nop 0\n"
    "global_load_lds_dwordx4 v67, s[40:41]\n"
    "s_add_u32 s40, s40, 0x4000\n"
    "s_addc_u32 s41, s41, 0\n"
    "s_sub_u32 s55, s55, 1\n"
    "s_cmp_eq_u32 s55, 0\n"
    "s_cselect_b32 s53, 28672, 0\n"
    "s_sub_u32 s46, s46, s53\n"
    "s_subb_u32 s47, s47, 0\n"
    "s_sub_u32 s48, s48, s53\n"
    "s_subb_u32 s49, s49, 0\n"
    "global_load_dwordx4 v[120:123], v68, s[46:47] sc1\n"
    "global_load_dwordx4 v[124:127], v68, s[48:49] sc1\n"
    "s_add_u32 s46, s46, 0x400\n"
    "s_addc_u32 s47, s47, 0\n"
    "s_add_u32 s48, s48, 0x400\n"
    "s_addc_u32 s49, s49, 0\n"
    "s_waitcnt vmcnt(24) lgkmcnt(0)\n"
    "ds_read_b64_tr_b16 v[72:73], v64 offset:12288\n"
    "ds_read_b64_tr_b16 v[74:75], v64 offset:13312\n"
    "ds_read_b64_tr_b16 v[76:77], v65 offset:12288\n"
    "ds_read_b64_tr_b16 v[78:79], v65 offset:13312\n"
    "v_mfma_f32_32x32x16_bf16 v[0:15], v[80:83], v[128:131], v[0:15]\n"
    "v_mfma_f32_32x32x16_bf16 v[16:31], v[80:83], v[132:135], v[16:31]\n"
    "v_mfma_f32_32x32x16_bf16 v[32:47], v[84:87], v[128:131], v[32:47]\n"
    "v_mfma_f32_32x32x16_bf16 v[48:63], v[84:87], v[132:135], v[48:63]\n"
    "s_add_u32 m0, s45, 10240\n"
    "s_nop 0\n"
    "global_load_lds_dwordx4 v66, s[40:41]\n"
    "s_add_u32 m0, s45, 11264\n"
    "s_nop 0\n"
    "global_load_lds_dwordx4 v67, s[40:41]\n"
    "s_add_u32 s40, s40, 0x4000\n"
    "s_addc_u32 s41, s41, 0\n"
    "global_load_dwordx4 v[128:131], v68, s[46:47] sc1\n"
    "global_load_dwordx4 v[132:135], v68, s[48:49] sc1\n"
    "s_add_u32 s46, s46, 0x400\n"
    "s_addc_u32 s47, s47, 0\n"
    "s_add_u32 s48, s48, 0x400\n"
    "s_addc_u32 s49, s49, 0\n"
    "s_waitcnt vmcnt(26) lgkmcnt(0)\n"
    "ds_read_b64_tr_b16 v[80:81], v64 offset:14336\n"
    "ds_read_b64_tr_b16 v[82:83], v64 offset:15360\n"
    "ds_read_b64_tr_b16 v[84:85], v65 offset:14336\n"
    "ds_read_b64_tr_b16 v[86:87], v65 offset:15360\n"
    "v_mfma_f32_32x32x16_bf16 v[0:15], v[72:75], v[136:139], v[0:15]\n"
    "v_mfma_f32_32x32x16_bf16 v[16:31], v[72:75], v[140:143], v[16:31]\n"
    "v_mfma_f32_32x32x16_bf16 v[32:47], v[76:79], v[136:139], v[32:47]\n"
    "v_mfma_f32_32x32x16_bf16 v[48:63], v[76:79], v[140:143], v[48:63]\n"
    "s_add_u32 m0, s45, 12288\n"
    "s_nop 0\n"
    "global_load_lds_dwordx4 v66, s[40:41]\n"
    "s_add_u32 m0, s45, 13312\n"
    "s_nop 0\n"
    "global_load_lds_dwordx4 v67, s[40:41]\n"
    "s_add_u32 s40, s40, 0x4000\n"
    "s_addc_u32 s41, s41, 0\n"
    "global_load_dwordx4 v[136:139], v68, s[46:47] sc1\n"
    "global_load_dwordx4 v[140:143], v68, s[48:49] sc1\n"
    "s_add_u32 s46, s46, 0x400\n"
    "s_addc_u32 s47, s47, 0\n"
    "s_add_u32 s48, s48, 0x400\n"
    "s_addc_u32 s49, s49, 0\n"
    "s_waitcnt vmcnt(26) lgkmcnt(0)\n"
    "ds_read_b64_tr_b16 v[72:73], v64 offset:0\n"
    "ds_read_b64_tr_b16 v[74:75], v64 offset:1024\n"
    "ds_read_b64_tr_b16 v[76:77], v65 offset:0\n"
    "ds_read_b64_tr_b16 v[78:79], v65 offset:1024\n"
    "v_mfma_f32_32x32x16_bf16 v[0:15], v[80:83], v[144:147], v[0:15]\n"
    "v_mfma_f32_32x32x16_bf16 v[16:31], v[80:83], v[148:151], v[16:31]\n"
    "v_mfma_f32_32x32x16_bf16 v[32:47], v[84:87], v[144:147], v[32:47]\n"
    "v_mfma_f32_32x32x16_bf16 v[48:63], v[84:87], v[148:151], v[48:63]\n"
    "s_add_u32 m0, s45, 14336\n"
    "s_nop 0\n"
    "global_load_lds_dwordx4 v66, s[40:41]\n"
    "s_add_u32 m0, s45, 15360\n"
    "s_nop 0\n"
    "global_load_lds_dwordx4 v67, s[40:41]\n"
    "s_add_u32 s40, s40, 0x4000\n"
    "s_addc_u32 s41, s41, 0\n"
    "global_load_dwordx4 v[144:147], v68, s[46:47] sc1\n"
    "global_load_dwordx4 v[148:151], v68, s[48:49] sc1\n"
    "s_add_u32 s46, s46, 0x400\n"
    "s_addc_u32 s47, s47, 0\n"
    "s_add_u32 s48, s48, 0x400\n"
    "s_addc_u32 s49, s49, 0\n"
    "s_waitcnt vmcnt(26) lgkmcnt(0)\n"
    "ds_read_b64_tr_b16 v[80:81], v64 offset:2048\n"
    "ds_read_b64_tr_b16 v[82:83], v64 offset:3072\n"
    "ds_read_b64_tr_b16 v[84:85], v65 offset:2048\n"
    "ds_read_b64_tr_b16 v[86:87], v65 offset:3072\n"
    "v_mfma_f32_32x32x16_bf16 v[0:15], v[72:75], v[88:91], v[0:15]\n"
    "v_mfma_f32_32x32x16_bf16 v[16:31], v[72:75], v[92:95], v[16:31]\n"
    "v_mfma_f32_32x32x16_bf16 v[32:47], v[76:79], v[88:91], v[32:47]\n"
    "v_mfma_f32_32x32x16_bf16 v[48:63], v[76:79], v[92:95], v[48:63]\n"
    "s_sub_u32 s51, s51, 1\n"
    "s_cmp_eq_u32 s51, 0\n"
    "s_cselect_b32 s52, 458752, 0\n"
    "s_sub_u32 s40, s40, s52\n"
    "s_subb_u32 s41, s41, 0\n"
    "s_add_u32 m0, s45, 0\n"
    "s_nop 0\n"
    "global_load_lds_dwordx4 v66, s[40:41]\n"
    "s_add_u32 m0, s45, 1024\n"
    "s_nop 0\n"
    "global_load_lds_dwordx4 v67, s[40:41]\n"
    "s_add_u32 s40, s40, 0x4000\n"
    "s_addc_u32 s41, s41, 0\n"
    "s_sub_u32 s55, s55, 1\n"
    "s_cmp_eq_u32 s55, 0\n"
    "s_cselect_b32 s53, 28672, 0\n"
    "s_sub_u32 s46, s46, s53\n"
    "s_subb_u32 s47, s47, 0\n"
    "s_sub_u32 s48, s48, s53\n"
    "s_subb_u32 s49, s49, 0\n"
    "global_load_dwordx4 v[88:91], v68, s[46:47] sc1\n"
    "global_load_dwordx4 v[92:95], v68, s[48:49] sc1\n"
    "s_add_u32 s46, s46, 0x400\n"
    "s_addc_u32 s47, s47, 0\n"
    "s_add_u32 s48, s48, 0x400\n"
    "s_addc_u32 s49, s49, 0\n"
    "s_waitcnt vmcnt(26) lgkmcnt(0)\n"
    "ds_read_b64_tr_b16 v[72:73], v64 offset:4096\n"
    "ds_read_b64_tr_b16 v[74:75], v64 offset:5120\n"
    "ds_read_b64_tr_b16 v[76:77], v65 offset:4096\n"
    "ds_read_b64_tr_b16 v[78:79], v65 offset:5120\n"
    "v_mfma_f32_32x32x16_bf16 v[0:15], v[80:83], v[96:99], v[0:15]\n"
    "v_mfma_f32_32x32x16_bf16 v[16:31], v[80:83], v[100:103], v[16:31]\n"
    "v_mfma_f32_32x32x16_bf16 v[32:47], v[84:87], v[96:99], v[32:47]\n"
    "v_mfma_f32_32x32x16_bf16 v[48:63], v[84:87], v[100:103], v[48:63]\n"
    "s_add_u32 m0, s45, 2048\n"
    "s_nop 0\n"
    "global_load_lds_dwordx4 v66, s[40:41]\n"
    "s_add_u32 m0, s45, 3072\n"
    "s_nop 0\n"
    "global_load_lds_dwordx4 v67, s[40:41]\n"
    "s_add_u32 s40, s40, 0x4000\n"
    "s_addc_u32 s41, s41, 0\n"
    "global_load_dwordx4 v[96:99], v68, s[46:47] sc1\n"
    "global_load_dwordx4 v[100:103], v68, s[48:49] sc1\n"
    "s_add_u32 s46, s46, 0x400\n"
    "s_addc_u32 s47, s47, 0\n"
    "s_add_u32 s48, s48, 0x400\n"
    "s_addc_u32 s49, s49, 0\n"
    "s_waitcnt vmcnt(26) lgkmcnt(0)\n"
    "ds_read_b64_tr_b16 v[80:81], v64 offset:6144\n"
    "ds_read_b64_tr_b16 v[82:83], v64 offset:7168\n"
    "ds_read_b64_tr_b16 v[84:85], v65 offset:6144\n"
    "ds_read_b64_tr_b16 v[86:87], v65 offset:7168\n"
    "v_mfma_f32_32x32x16_bf16 v[0:15], v[72:75], v[104:107], v[0:15]\n"
    "v_mfma_f32_32x32x16_bf16 v[16:31], v[72:75], v[108:111], v[16:31]\n"
    "v_mfma_f32_32x32x16_bf16 v[32:47], v[76:79], v[104:107], v[32:47]\n"
    "v_mfma_f32_32x32x16_bf16 v[48:63], v[76:79], v[108:111], v[48:63]\n"
    "s_add_u32 m0, s45, 4096\n"
    "s_nop 0\n"
    "global_load_lds_dwordx4 v66, s[40:41]\n"
    "s_add_u32 m0, s45, 5120\n"
    "s_nop 0\n"
    "global_load_lds_dwordx4 v67, s[40:41]\n"
    "s_add_u32 s40, s40, 0x4000\n"
    "s_addc_u32 s41, s41, 0\n"
    "global_load_dwordx4 v[104:107], v68, s[46:47] sc1\n"
    "global_load_dwordx4 v[108:111], v68, s[48:49] sc1\n"
    "s_add_u32 s46, s46, 0x400\n"
    "s_addc_u32 s47, s47, 0\n"
    "s_add_u32 s48, s48, 0x400\n"
    "s_addc_u32 s49, s49, 0\n"
    "s_waitcnt vmcnt(26) lgkmcnt(0)\n"
    "ds_read_b64_tr_b16 v[72:73], v64 offset:8192\n"
    "ds_read_b64_tr_b16 v[74:75], v64 offset:9216\n"
    "ds_read_b64_tr_b16 v[76:77], v65 offset:8192\n"
    "ds_read_b64_tr_b16 v[78:79], v65 offset:9216\n"
    "v_mfma_f32_32x32x16_bf16 v[0:15], v[80:83], v[112:115], v[0:15]\n"
    "v_mfma_f32_32x32x16_bf16 v[16:31], v[80:83], v[116:119], v[16:31]\n"
    "v_mfma_f32_32x32x16_bf16 v[32:47], v[84:87], v[112:115], v[32:47]\n"
    "v_mfma_f32_32x32x16_bf16 v[48:63], v[84:87], v[116:119], v[48:63]\n"
    "s_add_u32 m0, s45, 6144\n"
    "s_nop 0\n"
    "global_load_lds_dwordx4 v66, s[40:41]\n"
    "s_add_u32 m0, s45, 7168\n"
    "s_nop 0\n"
    "global_load_lds_dwordx4 v67, s[40:41]\n"
    "s_add_u32 s40, s40, 0x4000\n"
    "s_addc_u32 s41, s41, 0\n"
    "global_load_dwordx4 v[112:115], v68, s[46:47] sc1\n"
    "global_load_dwordx4 v[116:119], v68, s[48:49] sc1\n"
    "s_add_u32 s46, s46, 0x400\n"
    "s_addc_u32 s47, s47, 0\n"
    "s_add_u32 s48, s48, 0x400\n"
    "s_addc_u32 s49, s49, 0\n"
    "s_waitcnt vmcnt(26) lgkmcnt(0)\n"
    "ds_read_b64_tr_b16 v[80:81], v64 offset:10240\n"
    "ds_read_b64_tr_b16 v[82:83], v64 offset:11264\n"
    "ds_read_b64_tr_b16 v[84:85], v65 offset:10240\n"
    "ds_read_b64_tr_b16 v[86:87], v65 offset:11264\n"
    "v_mfma_f32_32x32x16_bf16 v[0:15], v[72:75], v[120:123], v[0:15]\n"
    "v_mfma_f32_32x32x16_bf16 v[16:31], v[72:75], v[124:127], v[16:31]\n"
    "v_mfma_f32_32x32x16_bf16 v[32:47], v[76:79], v[120:123], v[32:47]\n"
    "v_mfma_f32_32x32x16_bf16 v[48:63], v[76:79], v[124:127], v[48:63]\n"
    "s_sub_u32 s51, s51, 1\n"
    "s_cmp_eq_u32 s51, 0\n"
    "s_cselect_b32 s52, 458752, 0\n"
    "s_sub_u32 s40, s40, s52\n"
    "s_subb_u32 s41, s41, 0\n"
    "s_add_u32 m0, s45, 8192\n"
    "s_nop 0\n"
    "global_load_lds_dwordx4 v66, s[40:41]\n"
    "s_add_u32 m0, s45, 9216\n"
    "s_nop 0\n"
    "global_load_lds_dwordx4 v67, s[40:41]\n"
    "s_add_u32 s40, s40, 0x4000\n"
    "s_addc_u32 s41, s41, 0\n"
    "s_sub_u32 s55, s55, 1\n"
    "s_cmp_eq_u32 s55, 0\n"
    "s_cselect_b32 s53, 28672, 0\n"
    "s_sub_u32 s46, s46, s53\n"
    "s_subb_u32 s47, s47, 0\n"
    "s_sub_u32 s48, s48, s53\n"
    "s_subb_u32 s49, s49, 0\n"
    "global_load_dwordx4 v[120:123], v68, s[46:47] sc1\n"
    "global_load_dwordx4 v[124:127], v68, s[48:49] sc1\n"
    "s_add_u32 s46, s46, 0x400\n"
    "s_addc_u32 s47, s47, 0\n"
    "s_add_u32 s48, s48, 0x400\n"
    "s_addc_u32 s49, s49, 0\n"
    "s_waitcnt vmcnt(26) lgkmcnt(0)\n"
    "ds_read_b64_tr_b16 v[72:73], v64 offset:12288\n"
    "ds_read_b64_tr_b16 v[74:75], v64 offset:13312\n"
    "ds_read_b64_tr_b16 v[76:77], v65 offset:12288\n"
    "ds_read_b64_tr_b16 v[78:79], v65 offset:13312\n"
    "v_mfma_f32_32x32x16_bf16 v[0:15], v[80:83], v[128:131], v[0:15]\n"
    "v_mfma_f32_32x32x16_bf16 v[16:31], v[80:83], v[132:135], v[16:31]\n"
    "v_mfma_f32_32x32x16_bf16 v[32:47], v[84:87], v[128:131], v[32:47]\n"
    "v_mfma_f32_32x32x16_bf16 v[48:63], v[84:87], v[132:135], v[48:63]\n"
    "s_add_u32 m0, s45, 10240\n"
    "s_nop 0\n"
    "global_load_lds_dwordx4 v66, s[40:41]\n"
    "s_add_u32 m0, s45, 11264\n"
    "s_nop 0\n"
    "global_load_lds_dwordx4 v67, s[40:41]\n"
    "s_add_u32 s40, s40, 0x4000\n"
    "s_addc_u32 s41, s41, 0\n"
    "global_load_dwordx4 v[128:131], v68, s[46:47] sc1\n"
    "global_load_dwordx4 v[132:135], v68, s[48:49] sc1\n"
    "s_add_u32 s46, s46, 0x400\n"
    "s_addc_u32 s47, s47, 0\n"
    "s_add_u32 s48, s48, 0x400\n"
    "s_addc_u32 s49, s49, 0\n"
    "s_waitcnt vmcnt(26) lgkmcnt(0)\n"
    "ds_read_b64_tr_b16 v[80:81], v64 offset:14336\n"
    "ds_read_b64_tr_b16 v[82:83], v64 offset:15360\n"
    "ds_read_b64_tr_b16 v[84:85], v65 offset:14336\n"
    "ds_read_b64_tr_b16 v[86:87], v65 offset:15360\n"
    "v_mfma_f32_32x32x16_bf16 v[0:15], v[72:75], v[136:139], v[0:15]\n"
    "v_mfma_f32_32x32x16_bf16 v[16:31], v[72:75], v[140:143], v[16:31]\n"
    "v_mfma_f32_32x32x16_bf16 v[32:47], v[76:79], v[136:139], v[32:47]\n"
    "v_mfma_f32_32x32x16_bf16 v[48:63], v[76:79], v[140:143], v[48:63]\n"
    "s_add_u32 m0, s45, 12288\n"
    "s_nop 0\n"
    "global_load_lds_dwordx4 v66, s[40:41]\n"
    "s_add_u32 m0, s45, 13312\n"
    "s_nop 0\n"
    "global_load_lds_dwordx4 v67, s[40:41]\n"
    "s_add_u32 s40, s40, 0x4000\n"
    "s_addc_u32 s41, s41, 0\n"
    "global_load_dwordx4 v[136:139], v68, s[46:47] sc1\n"
    "global_load_dwordx4 v[140:143], v68, s[48:49] sc1\n"
    "s_add_u32 s46, s46, 0x400\n"
    "s_addc_u32 s47, s47, 0\n"
    "s_add_u32 s48, s48, 0x400\n"
    "s_addc_u32 s49, s49, 0\n"
    "s_waitcnt vmcnt(26) lgkmcnt(0)\n"
    "ds_read_b64_tr_b16 v[72:73], v64 offset:0\n"
    "ds_read_b64_tr_b16 v[74:75], v64 offset:1024\n"
    "ds_read_b64_tr_b16 v[76:77], v65 offset:0\n"
    "ds_read_b64_tr_b16 v[78:79], v65 offset:1024\n"
    "v_mfma_f32_32x32x16_bf16 v[0:15], v[80:83], v[144:147], v[0:15]\n"
    "v_mfma_f32_32x32x16_bf16 v[16:31], v[80:83], v[148:151], v[16:31]\n"
    "v_mfma_f32_32x32x16_bf16 v[32:47], v[84:87], v[144:147], v[32:47]\n"
    "v_mfma_f32_32x32x16_bf16 v[48:63], v[84:87], v[148:151], v[48:63]\n"
    "s_add_u32 m0, s45, 14336\n"
    "s_nop 0\n"
    "global_load_lds_dwordx4 v66, s[40:41]\n"
    "s_add_u32 m0, s45, 15360\n"
    "s_nop 0\n"
    "global_load_lds_dwordx4 v67, s[40:41]\n"
    "s_add_u32 s40, s40, 0x4000\n"
    "s_addc_u32 s41, s41, 0\n"
    "global_load_dwordx4 v[144:147], v68, s[46:47] sc1\n"
    "global_load_dwordx4 v[148:151], v68, s[48:49] sc1\n"
    "s_add_u32 s46, s46, 0x400\n"
    "s_addc_u32 s47, s47, 0\n"
    "s_add_u32 s48, s48, 0x400\n"
    "s_addc_u32 s49, s49, 0\n"
    "s_waitcnt vmcnt(26) lgkmcnt(0)\n"
    "ds_read_b64_tr_b16 v[80:81], v64 offset:2048\n"
    "ds_read_b64_tr_b16 v[82:83], v64 offset:3072\n"
    "ds_read_b64_tr_b16 v[84:85], v65 offset:2048\n"
    "ds_read_b64_tr_b16 v[86:87], v65 offset:3072\n"
    "v_mfma_f32_32x32x16_bf16 v[0:15], v[72:75], v[88:91], v[0:15]\n"
    "v_mfma_f32_32x32x16_bf16 v[16:31], v[72:75], v[92:95], v[16:31]\n"
    "v_mfma_f32_32x32x16_bf16 v[32:47], v[76:79], v[88:91], v[32:47]\n"
    "v_mfma_f32_32x32x16_bf16 v[48:63], v[76:79], v[92:95], v[48:63]\n"
    "s_sub_u32 s51, s51, 1\n"
    "s_cmp_eq_u32 s51, 0\n"
    "s_cselect_b32 s52, 458752, 0\n"
    "s_sub_u32 s40, s40, s52\n"
    "s_subb_u32 s41, s41, 0\n"
    "s_add_u32 m0, s45, 0\n"
    "s_nop 0\n"
    "global_load_lds_dwordx4 v66, s[40:41]\n"
    "s_add_u32 m0, s45, 1024\n"
    "s_nop 0\n"
    "global_load_lds_dwordx4 v67, s[40:41]\n"
    "s_add_u32 s40, s40, 0x4000\n"
    "s_addc_u32 s41, s41, 0\n"
    "s_sub_u32 s55, s55, 1\n"
    "s_cmp_eq_u32 s55, 0\n"
    "s_cselect_b32 s53, 28672, 0\n"
    "s_sub_u32 s46, s46, s53\n"
    "s_subb_u32 s47, s47, 0\n"
    "s_sub_u32 s48, s48, s53\n"
    "s_subb_u32 s49, s49, 0\n"
    "global_load_dwordx4 v[88:91], v68, s[46:47] sc1\n"
    "global_load_dwordx4 v[92:95], v68, s[48:49] sc1\n"
    "s_add_u32 s46, s46, 0x400\n"
    "s_addc_u32 s47, s47, 0\n"
    "s_add_u32 s48, s48, 0x400\n"
    "s_addc_u32 s49, s49, 0\n"
    "s_waitcnt vmcnt(26) lgkmcnt(0)\n"
    "ds_read_b64_tr_b16 v[72:73], v64 offset:4096\n"
    "ds_read_b64_tr_b16 v[74:75], v64 offset:5120\n"
    "ds_read_b64_tr_b16 v[76:77], v65 offset:4096\n"
    "ds_read_b64_tr_b16 v[78:79], v65 offset:5120\n"
    "v_mfma_f32_32x32x16_bf16 v[0:15], v[80:83], v[96:99], v[0:15]\n"
    "v_mfma_f32_32x32x16_bf16 v[16:31], v[80:83], v[100:103], v[16:31]\n"
    "v_mfma_f32_32x32x16_bf16 v[32:47], v[84:87], v[96:99], v[32:47]\n"
    "v_mfma_f32_32x32x16_bf16 v[48:63], v[84:87], v[100:103], v[48:63]\n"
    "s_add_u32 m0, s45, 2048\n"
    "s_nop 0\n"
    "global_load_lds_dwordx4 v66, s[40:41]\n"
    "s_add_u32 m0, s45, 3072\n"
    "s_nop 0\n"
    "global_load_lds_dwordx4 v67, s[40:41]\n"
    "s_add_u32 s40, s40, 0x4000\n"
    "s_addc_u32 s41, s41, 0\n"
    "global_load_dwordx4 v[96:99], v68, s[46:47] sc1\n"
    "global_load_dwordx4 v[100:103], v68, s[48:49] sc1\n"
    "s_add_u32 s46, s46, 0x400\n"
    "s_addc_u32 s47, s47, 0\n"
    "s_add_u32 s48, s48, 0x400\n"
    "s_addc_u32 s49, s49, 0\n"
    "s_waitcnt vmcnt(26) lgkmcnt(0)\n"
    "ds_read_b64_tr_b16 v[80:81], v64 offset:6144\n"
    "ds_read_b64_tr_b16 v[82:83], v64 offset:7168\n"
    "ds_read_b64_tr_b16 v[84:85], v65 offset:6144\n"
    "ds_read_b64_tr_b16 v[86:87], v65 offset:7168\n"
    "v_mfma_f32_32x32x16_bf16 v[0:15], v[72:75], v[104:107], v[0:15]\n"
    "v_mfma_f32_32x32x16_bf16 v[16:31], v[72:75], v[108:111], v[16:31]\n"
    "v_mfma_f32_32x32x16_bf16 v[32:47], v[76:79], v[104:107], v[32:47]\n"
    "v_mfma_f32_32x32x16_bf16 v[48:63], v[76:79], v[108:111], v[48:63]\n"
    "s_add_u32 m0, s45, 4096\n"
    "s_nop 0\n"
    "global_load_lds_dwordx4 v66, s[40:41]\n"
    "s_add_u32 m0, s45, 5120\n"
    "s_nop 0\n"
    "global_load_lds_dwordx4 v67, s[40:41]\n"
    "s_add_u32 s40, s40, 0x4000\n"
    "s_addc_u32 s41, s41, 0\n"
    "global_load_dwordx4 v[104:107], v68, s[46:47] sc1\n"
    "global_load_dwordx4 v[108:111], v68, s[48:49] sc1\n"
    "s_add_u32 s46, s46, 0x400\n"
    "s_addc_u32 s47, s47, 0\n"
    "s_add_u32 s48, s48, 0x400\n"
    "s_addc_u32 s49, s49, 0\n"
    "s_waitcnt vmcnt(26) lgkmcnt(0)\n"
    "ds_read_b64_tr_b16 v[72:73], v64 offset:8192\n"
    "ds_read_b64_tr_b16 v[74:75], v64 offset:9216\n"
    "ds_read_b64_tr_b16 v[76:77], v65 offset:8192\n"
    "ds_read_b64_tr_b16 v[78:79], v65 offset:9216\n"
    "v_mfma_f32_32x32x16_bf16 v[0:15], v[80:83], v[112:115], v[0:15]\n"
    "v_mfma_f32_32x32x16_bf16 v[16:31], v[80:83], v[116:119], v[16:31]\n"
    "v_mfma_f32_32x32x16_bf16 v[32:47], v[84:87], v[112:115], v[32:47]\n"
    "v_mfma_f32_32x32x16_bf16 v[48:63], v[84:87], v[116:119], v[48:63]\n"
    "s_add_u32 m0, s45, 6144\n"
    "s_nop 0\n"
    "global_load_lds_dwordx4 v66, s[40:41]\n"
    "s_add_u32 m0, s45, 7168\n"
    "s_nop 0\n"
    "global_load_lds_dwordx4 v67, s[40:41]\n"
    "s_add_u32 s40, s40, 0x4000\n"
    "s_addc_u32 s41, s41, 0\n"
    "global_load_dwordx4 v[112:115], v68, s[46:47] sc1\n"
    "global_load_dwordx4 v[116:119], v68, s[48:49] sc1\n"
    "s_add_u32 s46, s46, 0x400\n"
    "s_addc_u32 s47, s47, 0\n"
    "s_add_u32 s48, s48, 0x400\n"
    "s_addc_u32 s49, s49, 0\n"
    "s_waitcnt vmcnt(26) lgkmcnt(0)\n"
    "ds_read_b64_tr_b16 v[80:81], v64 offset:10240\n"
    "ds_read_b64_tr_b16 v[82:83], v64 offset:11264\n"
    "ds_read_b64_tr_b16 v[84:85], v65 offset:10240\n"
    "ds_read_b64_tr_b16 v[86:87], v65 offset:11264\n"
    "v_mfma_f32_32x32x16_bf16 v[0:15], v[72:75], v[120:123], v[0:15]\n"
    "v_mfma_f32_32x32x16_bf16 v[16:31], v[72:75], v[124:127], v[16:31]\n"
    "v_mfma_f32_32x32x16_bf16 v[32:47], v[76:79], v[120:123], v[32:47]\n"
    "v_mfma_f32_32x32x16_bf16 v[48:63], v[76:79], v[124:127], v[48:63]\n"
    "s_waitcnt vmcnt(22) lgkmcnt(0)\n"
    "ds_read_b64_tr_b16 v[72:73], v64 offset:12288\n"
    "ds_read_b64_tr_b16 v[74:75], v64 offset:13312\n"
    "ds_read_b64_tr_b16 v[76:77], v65 offset:12288\n"
    "ds_read_b64_tr_b16 v[78:79], v65 offset:13312\n"
    "v_mfma_f32_32x32x16_bf16 v[0:15], v[80:83], v[128:131], v[0:15]\n"
    "v_mfma_f32_32x32x16_bf16 v[16:31], v[80:83], v[132:135], v[16:31]\n"
    "v_mfma_f32_32x32x16_bf16 v[32:47], v[84:87], v[128:131], v[32:47]\n"
    "v_mfma_f32_32x32x16_bf16 v[48:63], v[84:87], v[132:135], v[48:63]\n"
    "s_waitcnt vmcnt(18) lgkmcnt(0)\n"
    "ds_read_b64_tr_b16 v[80:81], v64 offset:14336\n"
    "ds_read_b64_tr_b16 v[82:83], v64 offset:15360\n"
    "ds_read_b64_tr_b16 v[84:85], v65 offset:14336\n"
    "ds_read_b64_tr_b16 v[86:87], v65 offset:15360\n"
    "v_mfma_f32_32x32x16_bf16 v[0:15], v[72:75], v[136:139], v[0:15]\n"
    "v_mfma_f32_32x32x16_bf16 v[16:31], v[72:75], v[140:143], v[16:31]\n"
    "v_mfma_f32_32x32x16_bf16 v[32:47], v[76:79], v[136:139], v[32:47]\n"
    "v_mfma_f32_32x32x16_bf16 v[48:63], v[76:79], v[140:143], v[48:63]\n"
    "s_waitcnt vmcnt(14) lgkmcnt(0)\n"
    "ds_read_b64_tr_b16 v[72:73], v64 offset:0\n"
    "ds_read_b64_tr_b16 v[74:75], v64 offset:1024\n"
    "ds_read_b64_tr_b16 v[76:77], v65 offset:0\n"
    "ds_read_b64_tr_b16 v[78:79], v65 offset:1024\n"
    "v_mfma_f32_32x32x16_bf16 v[0:15], v[80:83], v[144:147], v[0:15]\n"
    "v_mfma_f32_32x32x16_bf16 v[16:31], v[80:83], v[148:151], v[16:31]\n"
    "v_mfma_f32_32x32x16_bf16 v[32:47], v[84:87], v[144:147], v[32:47]\n"
    "v_mfma_f32_32x32x16_bf16 v[48:63], v[84:87], v[148:151], v[48:63]\n"
    "s_waitcnt vmcnt(10) lgkmcnt(0)\n"
    "ds_read_b64_tr_b16 v[80:81], v64 offset:2048\n"
    "ds_read_b64_tr_b16 v[82:83], v64 offset:3072\n"
    "ds_read_b64_tr_b16 v[84:85], v65 offset:2048\n"
    "ds_read_b64_tr_b16 v[86:87], v65 offset:3072\n"
    "v_mfma_f32_32x32x16_bf16 v[0:15], v[72:75], v[88:91], v[0:15]\n"
    "v_mfma_f32_32x32x16_bf16 v[16:31], v[72:75], v[92:95], v[16:31]\n"
    "v_mfma_f32_32x32x16_bf16 v[32:47], v[76:79], v[88:91], v[32:47]\n"
    "v_mfma_f32_32x32x16_bf16 v[48:63], v[76:79], v[92:95], v[48:63]\n"
    "s_waitcnt vmcnt(6) lgkmcnt(0)\n"
    "ds_read_b64_tr_b16 v[72:73], v64 offset:4096\n"
    "ds_read_b64_tr_b16 v[74:75], v64 offset:5120\n"
    "ds_read_b64_tr_b16 v[76:77], v65 offset:4096\n"
    "ds_read_b64_tr_b16 v[78:79], v65 offset:5120\n"
    "v_mfma_f32_32x32x16_bf16 v[0:15], v[80:83], v[96:99], v[0:15]\n"
    "v_mfma_f32_32x32x16_bf16 v[16:31], v[80:83], v[100:103], v[16:31]\n"
    "v_mfma_f32_32x32x16_bf16 v[32:47], v[84:87], v[96:99], v[32:47]\n"
    "v_mfma_f32_32x32x16_bf16 v[48:63], v[84:87], v[100:103], v[48:63]\n"
    "s_waitcnt vmcnt(2) lgkmcnt(0)\n"
    "ds_read_b64_tr_b16 v[80:81], v64 offset:6144\n"
    "ds_read_b64_tr_b16 v[82:83], v64 offset:7168\n"
    "ds_read_b64_tr_b16 v[84:85], v65 offset:6144\n"
    "ds_read_b64_tr_b16 v[86:87], v65 offset:7168\n"
    "v_mfma_f32_32x32x16_bf16 v[0:15], v[72:75], v[104:107], v[0:15]\n"
    "v_mfma_f32_32x32x16_bf16 v[16:31], v[72:75], v[108:111], v[16:31]\n"
    "v_mfma_f32_32x32x16_bf16 v[32:47], v[76:79], v[104:107], v[32:47]\n"
    "v_mfma_f32_32x32x16_bf16 v[48:63], v[76:79], v[108:111], v[48:63]\n"
    "s_waitcnt vmcnt(0) lgkmcnt(0)\n"
    "v_mfma_f32_32x32x16_bf16 v[0:15], v[80:83], v[112:115], v[0:15]\n"
    "v_mfma_f32_32x32x16_bf16 v[16:31], v[80:83], v[116:119], v[16:31]\n"
    "v_mfma_f32_32x32x16_bf16 v[32:47], v[84:87], v[112:115], v[32:47]\n"
    "v_mfma_f32_32x32x16_bf16 v[48:63], v[84:87], v[116:119], v[48:63]\n"
    "s_setprio 2\n"
    "s_nop 7\n"
    "s_nop 7\n"
    : "={v[0:15]}"(a00), "={v[16:31]}"(a01), "={v[32:47]}"(a10), "={v[48:63]}"(a11),
      "+{s[40:41]}"(w), "+{s[46:47]}"(b0), "+{s[48:49]}"(b1), "+{s51}"(wrapw), "+{s55}"(wrapb)
    : "{v64}"(va0), "{v65}"(va1), "{v66}"(vw0), "{v67}"(vw1), "{v68}"(voff), "{s45}"(ring)
    : "v72", "v73", "v74", "v75", "v76", "v77", "v78", "v79", "v80", "v81", "v82", "v83", "v84", "v85", "v86", "v87", "v88", "v89", "v90", "v91", "v92", "v93", "v94", "v95", "v96", "v97", "v98", "v99", "v100", "v101", "v102", "v103", "v104", "v105", "v106", "v107", "v108", "v109", "v110", "v111", "v112", "v113", "v114", "v115", "v116", "v117", "v118", "v119", "v120", "v121", "v122", "v123", "v124", "v125", "v126", "v127", "v128", "v129", "v130", "v131", "v132", "v133", "v134", "v135", "v136", "v137", "v138", "v139", "v140", "v141", "v142", "v143", "v144", "v145", "v146", "v147", "v148", "v149", "v150", "v151", "s52", "s53", "scc", "memory");
}


constexpr int ATTN_U = 2;
DI void attn_loop(f32x16 (&o)[8], const bf16x8 (&q)[8], const void* kvbase, unsigned voff, unsigned iters) {
  unsigned long long p = (unsigned long long)kvbase;
  asm volatile(
    "global_load_dwordx4 v[200:203], v224, s[40:41] offset:0 sc1\n"
    "global_load_dwordx4 v[204:207], v224, s[40:41] offset:1024 sc1\n"
    "global_load_dwordx4 v[208:211], v224, s[40:41] offset:2048 sc1\n"
    "s_add_u32 s40, s40, 3072\n"
    "s_addc_u32 s41, s41, 0\n"
    "v_mov_b32 v0, 0\n"
    "v_mov_b32 v1, 0\n"
    "v_mov_b32 v2, 0\n"
    "v_mov_b32 v3, 0\n"
    "v_mov_b32 v4, 0\n"
    "v_mov_b32 v5, 0\n"
    "v_mov_b32 v6, 0\n"
    "v_mov_b32 v7, 0\n"
    "v_mov_b32 v8, 0\n"
    "v_mov_b32 v9, 0\n"
    "v_mov_b32 v10, 0\n"
    "v_mov_b32 v11, 0\n"
    "v_mov_b32 v12, 0\n"
    "v_mov_b32 v13, 0\n"
    "v_mov_b32 v14, 0\n"
    "v_mov_b32 v15, 0\n"
    "v_mov_b32 v16, 0\n"
    "v_mov_b32 v17, 0\n"
    "v_mov_b32 v18, 0\n"
    "v_mov_b32 v19, 0\n"
    "v_mov_b32 v20, 0\n"
    "v_mov_b32 v21, 0\n"
    "v_mov_b32 v22, 0\n"
    "v_mov_b32 v23, 0\n"
    "v_mov_b32 v24, 0\n"
    "v_mov_b32 v25, 0\n"
    "v_mov_b32 v26, 0\n"
    "v_mov_b32 v27, 0\n"
    "v_mov_b32 v28, 0\n"
    "v_mov_b32 v29, 0\n"
    "v_mov_b32 v30, 0\n"
    "v_mov_b32 v31, 0\n"
    "v_mov_b32 v32, 0\n"
    "v_mov_b32 v33, 0\n"
    "v_mov_b32 v34, 0\n"
    "v_mov_b32 v35, 0\n"
    "v_mov_b32 v36, 0\n"
    "v_mov_b32 v37, 0\n"
    "v_mov_b32 v38, 0\n"
    "v_mov_b32 v39, 0\n"
    "v_mov_b32 v40, 0\n"
    "v_mov_b32 v41, 0\n"
    "v_mov_b32 v42, 0\n"
    "v_mov_b32 v43, 0\n"
    "v_mov_b32 v44, 0\n"
    "v_mov_b32 v45, 0\n"
    "v_mov_b32 v46, 0\n"
    "v_mov_b32 v47, 0\n"
    "v_mov_b32 v48, 0\n"
    "v_mov_b32 v49, 0\n"
    "v_mov_b32 v50, 0\n"
    "v_mov_b32 v51, 0\n"
    "v_mov_b32 v52, 0\n"
    "v_mov_b32 v53, 0\n"
    "v_mov_b32 v54, 0\n"
    "v_mov_b32 v55, 0\n"
    "v_mov_b32 v56, 0\n"
    "v_mov_b32 v57, 0\n"
    "v_mov_b32 v58, 0\n"
    "v_mov_b32 v59, 0\n"
    "v_mov_b32 v60, 0\n"
    "v_mov_b32 v61, 0\n"
    "v_mov_b32 v62, 0\n"
    "v_mov_b32 v63, 0\n"
    "v_mov_b32 v64, 0\n"
    "v_mov_b32 v65, 0\n"
    "v_mov_b32 v66, 0\n"
    "v_mov_b32 v67, 0\n"
    "v_mov_b32 v68, 0\n"
    "v_mov_b32 v69, 0\n"
    "v_mov_b32 v70, 0\n"
    "v_mov_b32 v71, 0\n"
    "v_mov_b32 v72, 0\n"
    "v_mov_b32 v73, 0\n"
    "v_mov_b32 v74, 0\n"
    "v_mov_b32 v75, 0\n"
    "v_mov_b32 v76, 0\n"
    "v_mov_b32 v77, 0\n"
    "v_mov_b32 v78, 0\n"
    "v_mov_b32 v79, 0\n"
    "v_mov_b32 v80, 0\n"
    "v_mov_b32 v81, 0\n"
    "v_mov_b32 v82, 0\n"
    "v_mov_b32 v83, 0\n"
    "v_mov_b32 v84, 0\n"
    "v_mov_b32 v85, 0\n"
    "v_mov_b32 v86, 0\n"
    "v_mov_b32 v87, 0\n"
    "v_mov_b32 v88, 0\n"
    "v_mov_b32 v89, 0\n"
    "v_mov_b32 v90, 0\n"
    "v_mov_b32 v91, 0\n"
    "v_mov_b32 v92, 0\n"
    "v_mov_b32 v93, 0\n"
    "v_mov_b32 v94, 0\n"
    "v_mov_b32 v95, 0\n"
    "v_mov_b32 v96, 0\n"
    "v_mov_b32 v97, 0\n"
    "v_mov_b32 v98, 0\n"
    "v_mov_b32 v99, 0\n"
    "v_mov_b32 v100, 0\n"
    "v_mov_b32 v101, 0\n"
    "v_mov_b32 v102, 0\n"
    "v_mov_b32 v103, 0\n"
    "v_mov_b32 v104, 0\n"
    "v_mov_b32 v105, 0\n"
    "v_mov_b32 v106, 0\n"
    "v_mov_b32 v107, 0\n"
    "v_mov_b32 v108, 0\n"
    "v_mov_b32 v109, 0\n"
    "v_mov_b32 v110, 0\n"
    "v_mov_b32 v111, 0\n"
    "v_mov_b32 v112, 0\n"
    "v_mov_b32 v113, 0\n"
    "v_mov_b32 v114, 0\n"
    "v_mov_b32 v115, 0\n"
    "v_mov_b32 v116, 0\n"
    "v_mov_b32 v117, 0\n"
    "v_mov_b32 v118, 0\n"
    "v_mov_b32 v119, 0\n"
    "v_mov_b32 v120, 0\n"
    "v_mov_b32 v121, 0\n"
    "v_mov_b32 v122, 0\n"
    "v_mov_b32 v123, 0\n"
    "v_mov_b32 v124, 0\n"
    "v_mov_b32 v125, 0\n"
    "v_mov_b32 v126, 0\n"
    "v_mov_b32 v127, 0\n"
    "v_mov_b32 v196, 0\n"
    "v_mov_b32 v197, 0\n"
    "v_mov_b32 v198, 0\n"
    "v_mov_b32 v199, 0\n"
    "v_mov_b32 v190, 0\n"
    "v_mov_b32 v191, 0\n"
    "v_mov_b32 v220, 0\n"
    "v_mov_b32 v221, 0\n"
    "v_mov_b32 v222, 0\n"
    "v_mov_b32 v223, 0\n"
    "s_waitcnt vmcnt(2)\n"
    "v_mfma_f32_32x32x16_bf16 v[160:175], v[200:203], v[128:131], 0\n"
    "s_nop 7\n"
    "s_nop 3\n"
    "1:\n"
    "v_exp_f32 v160, v160\n"
    "v_exp_f32 v161, v161\n"
    "v_cvt_pk_bf16_f32 v199, v190, v191\n"
    "v_exp_f32 v162, v162\n"
    "v_exp_f32 v163, v163\n"
    "v_cvt_pk_bf16_f32 v192, v160, v161\n"
    "v_exp_f32 v164, v164\n"
    "v_mfma_f32_32x32x16_bf16 v[112:127], v[220:223], v[196:199], v[112:127]\n"
    "s_waitcnt vmcnt(0)\n"
    "global_load_dwordx4 v[212:215], v224, s[40:41] offset:0 sc1\n"
    "global_load_dwordx4 v[216:219], v224, s[40:41] offset:1024 sc1\n"
    "global_load_dwordx4 v[220:223], v224, s[40:41] offset:2048 sc1\n"
    "s_add_u32 s40, s40, 3072\n"
    "s_addc_u32 s41, s41, 0\n"
    "v_exp_f32 v165, v165\n"
    "v_cvt_pk_bf16_f32 v193, v162, v163\n"
    "v_exp_f32 v166, v166\n"
    "v_exp_f32 v167, v167\n"
    "v_mfma_f32_32x32x16_bf16 v[176:191], v[200:203], v[132:135], 0\n"
    "v_cvt_pk_bf16_f32 v194, v164, v165\n"
    "v_cvt_pk_bf16_f32 v195, v166, v167\n"
    "v_exp_f32 v168, v168\n"
    "v_exp_f32 v169, v169\n"
    "v_mfma_f32_32x32x16_bf16 v[0:15], v[204:207], v[192:195], v[0:15]\n"
    "v_cvt_pk_bf16_f32 v196, v168, v169\n"
    "v_exp_f32 v170, v170\n"
    "v_exp_f32 v171, v171\n"
    "v_exp_f32 v172, v172\n"
    "v_cvt_pk_bf16_f32 v197, v170, v171\n"
    "v_exp_f32 v173, v173\n"
    "v_exp_f32 v174, v174\n"
    "v_cvt_pk_bf16_f32 v198, v172, v173\n"
    "v_exp_f32 v175, v175\n"
    "v_exp_f32 v176, v176\n"
    "v_exp_f32 v177, v177\n"
    "v_cvt_pk_bf16_f32 v199, v174, v175\n"
    "v_exp_f32 v178, v178\n"
    "v_exp_f32 v179, v179\n"
    "v_cvt_pk_bf16_f32 v192, v176, v177\n"
    "v_exp_f32 v180, v180\n"
    "v_mfma_f32_32x32x16_bf16 v[0:15], v[208:211], v[196:199], v[0:15]\n"
    "v_exp_f32 v181, v181\n"
    "v_cvt_pk_bf16_f32 v193, v178, v179\n"
    "v_exp_f32 v182, v182\n"
    "v_exp_f32 v183, v183\n"
    "v_mfma_f32_32x32x16_bf16 v[160:175], v[200:203], v[136:139], 0\n"
    "v_cvt_pk_bf16_f32 v194, v180, v181\n"
    "v_cvt_pk_bf16_f32 v195, v182, v183\n"
    "v_exp_f32 v184, v184\n"
    "v_exp_f32 v185, v185\n"
    "v_mfma_f32_32x32x16_bf16 v[16:31], v[204:207], v[192:195], v[16:31]\n"
    "v_cvt_pk_bf16_f32 v196, v184, v185\n"
    "v_exp_f32 v186, v186\n"
    "v_exp_f32 v187, v187\n"
    "v_exp_f32 v188, v188\n"
    "v_cvt_pk_bf16_f32 v197, v186, v187\n"
    "v_exp_f32 v189, v189\n"
    "v_exp_f32 v190, v190\n"
    "v_cvt_pk_bf16_f32 v198, v188, v189\n"
    "v_exp_f32 v191, v191\n"
    "v_exp_f32 v160, v160\n"
    "v_exp_f32 v161, v161\n"
    "v_cvt_pk_bf16_f32 v199, v190, v191\n"
    "v_exp_f32 v162, v162\n"
    "v_exp_f32 v163, v163\n"
    "v_cvt_pk_bf16_f32 v192, v160, v161\n"
    "v_exp_f32 v164, v164\n"
    "v_mfma_f32_32x32x16_bf16 v[16:31], v[208:211], v[196:199], v[16:31]\n"
    "v_exp_f32 v165, v165\n"
    "v_cvt_pk_bf16_f32 v193, v162, v163\n"
    "v_exp_f32 v166, v166\n"
    "v_exp_f32 v167, v167\n"
    "v_mfma_f32_32x32x16_bf16 v[176:191], v[200:203], v[140:143], 0\n"
    "v_cvt_pk_bf16_f32 v194, v164, v165\n"
    "v_cvt_pk_bf16_f32 v195, v166, v167\n"
    "v_exp_f32 v168, v168\n"
    "v_exp_f32 v169, v169\n"
    "v_mfma_f32_32x32x16_bf16 v[32:47], v[204:207], v[192:195], v[32:47]\n"
    "v_cvt_pk_bf16_f32 v196, v168, v169\n"
    "v_exp_f32 v170, v170\n"
    "v_exp_f32 v171, v171\n"
    "v_exp_f32 v172, v172\n"
    "v_cvt_pk_bf16_f32 v197, v170, v171\n"
    "v_exp_f32 v173, v173\n"
    "v_exp_f32 v174, v174\n"
    "v_cvt_pk_bf16_f32 v198, v172, v173\n"
    "v_exp_f32 v175, v175\n"
    "v_exp_f32 v176, v176\n"
    "v_exp_f32 v177, v177\n"
    "v_cvt_pk_bf16_f32 v199, v174, v175\n"
    "v_exp_f32 v178, v178\n"
    "v_exp_f32 v179, v179\n"
    "v_cvt_pk_bf16_f32 v192, v176, v177\n"
    "v_exp_f32 v180, v180\n"
    "v_mfma_f32_32x32x16_bf16 v[32:47], v[208:211], v[196:199], v[32:47]\n"
    "v_exp_f32 v181, v181\n"
    "v_cvt_pk_bf16_f32 v193, v178, v179\n"
    "v_exp_f32 v182, v182\n"
    "v_exp_f32 v183, v183\n"
    "v_mfma_f32_32x32x16_bf16 v[160:175], v[200:203], v[144:147], 0\n"
    "v_cvt_pk_bf16_f32 v194, v180, v181\n"
    "v_cvt_pk_bf16_f32 v195, v182, v183\n"
    "v_exp_f32 v184, v184\n"
    "v_exp_f32 v185, v185\n"
    "v_mfma_f32_32x32x16_bf16 v[48:63], v[204:207], v[192:195], v[48:63]\n"
    "v_cvt_pk_bf16_f32 v196, v184, v185\n"
    "v_exp_f32 v186, v186\n"
    "v_exp_f32 v187, v187\n"
    "v_exp_f32 v188, v188\n"
    "v_cvt_pk_bf16_f32 v197, v186, v187\n"
    "v_exp_f32 v189, v189\n"
    "v_exp_f32 v190, v190\n"
    "v_cvt_pk_bf16_f32 v198, v188, v189\n"
    "v_exp_f32 v191, v191\n"
    "v_exp_f32 v160, v160\n"
    "v_exp_f32 v161, v161\n"
    "v_cvt_pk_bf16_f32 v199, v190, v191\n"
    "v_exp_f32 v162, v162\n"
    "v_exp_f32 v163, v163\n"
    "v_cvt_pk_bf16_f32 v192, v160, v161\n"
    "v_exp_f32 v164, v164\n"
    "v_mfma_f32_32x32x16_bf16 v[48:63], v[208:211], v[196:199], v[48:63]\n"
    "v_exp_f32 v165, v165\n"
    "v_cvt_pk_bf16_f32 v193, v162, v163\n"
    "v_exp_f32 v166, v166\n"
    "v_exp_f32 v167, v167\n"
    "v_mfma_f32_32x32x16_bf16 v[176:191], v[200:203], v[148:151], 0\n"
    "v_cvt_pk_bf16_f32 v194, v164, v165\n"
    "v_cvt_pk_bf16_f32 v195, v166, v167\n"
    "v_exp_f32 v168, v168\n"
    "v_exp_f32 v169, v169\n"
    "v_mfma_f32_32x32x16_bf16 v[64:79], v[204:207], v[192:195], v[64:79]\n"
    "v_cvt_pk_bf16_f32 v196, v168, v169\n"
    "v_exp_f32 v170, v170\n"
    "v_exp_f32 v171, v171\n"
    "v_exp_f32 v172, v172\n"
    "v_cvt_pk_bf16_f32 v197, v170, v171\n"
    "v_exp_f32 v173, v173\n"
    "v_exp_f32 v174, v174\n"
    "v_cvt_pk_bf16_f32 v198, v172, v173\n"
    "v_exp_f32 v175, v175\n"
    "v_exp_f32 v176, v176\n"
    "v_exp_f32 v177, v177\n"
    "v_cvt_pk_bf16_f32 v199, v174, v175\n"
    "v_exp_f32 v178, v178\n"
    "v_exp_f32 v179, v179\n"
    "v_cvt_pk_bf16_f32 v192, v176, v177\n"
    "v_exp_f32 v180, v180\n"
    "v_mfma_f32_32x32x16_bf16 v[64:79], v[208:211], v[196:199], v[64:79]\n"
    "v_exp_f32 v181, v181\n"
    "v_cvt_pk_bf16_f32 v193, v178, v179\n"
    "v_exp_f32 v182, v182\n"
    "v_exp_f32 v183, v183\n"
    "v_mfma_f32_32x32x16_bf16 v[160:175], v[200:203], v[152:155], 0\n"
    "v_cvt_pk_bf16_f32 v194, v180, v181\n"
    "v_cvt_pk_bf16_f32 v195, v182, v183\n"
    "v_exp_f32 v184, v184\n"
    "v_exp_f32 v185, v185\n"
    "v_mfma_f32_32x32x16_bf16 v[80:95], v[204:207], v[192:195], v[80:95]\n"
    "v_cvt_pk_bf16_f32 v196, v184, v185\n"
    "v_exp_f32 v186, v186\n"
    "v_exp_f32 v187, v187\n"
    "v_exp_f32 v188, v188\n"
    "v_cvt_pk_bf16_f32 v197, v186, v187\n"
    "v_exp_f32 v189, v189\n"
    "v_exp_f32 v190, v190\n"
    "v_cvt_pk_bf16_f32 v198, v188, v189\n"
    "v_exp_f32 v191, v191\n"
    "v_exp_f32 v160, v160\n"
    "v_exp_f32 v161, v161\n"
    "v_cvt_pk_bf16_f32 v199, v190, v191\n"
    "v_exp_f32 v162, v162\n"
    "v_exp_f32 v163, v163\n"
    "v_cvt_pk_bf16_f32 v192, v160, v161\n"
    "v_exp_f32 v164, v164\n"
    "v_mfma_f32_32x32x16_bf16 v[80:95], v[208:211], v[196:199], v[80:95]\n"
    "v_exp_f32 v165, v165\n"
    "v_cvt_pk_bf16_f32 v193, v162, v163\n"
    "v_exp_f32 v166, v166\n"
    "v_exp_f32 v167, v167\n"
    "v_mfma_f32_32x32x16_bf16 v[176:191], v[200:203], v[156:159], 0\n"
    "v_cvt_pk_bf16_f32 v194, v164, v165\n"
    "v_cvt_pk_bf16_f32 v195, v166, v167\n"
    "v_exp_f32 v168, v168\n"
    "v_exp_f32 v169, v169\n"
    "v_mfma_f32_32x32x16_bf16 v[96:111], v[204:207], v[192:195], v[96:111]\n"
    "v_cvt_pk_bf16_f32 v196, v168, v169\n"
    "v_exp_f32 v170, v170\n"
    "v_exp_f32 v171, v171\n"
    "v_exp_f32 v172, v172\n"
    "v_cvt_pk_bf16_f32 v197, v170, v171\n"
    "v_exp_f32 v173, v173\n"
    "v_exp_f32 v174, v174\n"
    "v_cvt_pk_bf16_f32 v198, v172, v173\n"
    "v_exp_f32 v175, v175\n"
    "v_exp_f32 v176, v176\n"
    "v_exp_f32 v177, v177\n"
    "v_cvt_pk_bf16_f32 v199, v174, v175\n"
    "v_exp_f32 v178, v178\n"
    "v_exp_f32 v179, v179\n"
    "v_cvt_pk_bf16_f32 v192, v176, v177\n"
    "v_exp_f32 v180, v180\n"
    "v_mfma_f32_32x32x16_bf16 v[96:111], v[208:211], v[196:199], v[96:111]\n"
    "v_exp_f32 v181, v181\n"
    "v_cvt_pk_bf16_f32 v193, v178, v179\n"
    "v_exp_f32 v182, v182\n"
    "v_exp_f32 v183, v183\n"
    "s_waitcnt vmcnt(2)\n"
    "v_mfma_f32_32x32x16_bf16 v[160:175], v[212:215], v[128:131], 0\n"
    "v_cvt_pk_bf16_f32 v194, v180, v181\n"
    "v_cvt_pk_bf16_f32 v195, v182, v183\n"
    "v_exp_f32 v184, v184\n"
    "v_exp_f32 v185, v185\n"
    "v_mfma_f32_32x32x16_bf16 v[112:127], v[204:207], v[192:195], v[112:127]\n"
    "v_cvt_pk_bf16_f32 v196, v184, v185\n"
    "v_exp_f32 v186, v186\n"
    "v_exp_f32 v187, v187\n"
    "v_exp_f32 v188, v188\n"
    "v_cvt_pk_bf16_f32 v197, v186, v187\n"
    "v_exp_f32 v189, v189\n"
    "v_exp_f32 v190, v190\n"
    "v_cvt_pk_bf16_f32 v198, v188, v189\n"
    "v_exp_f32 v191, v191\n"
    "v_exp_f32 v160, v160\n"
    "v_exp_f32 v161, v161\n"
    "v_cvt_pk_bf16_f32 v199, v190, v191\n"
    "v_exp_f32 v162, v162\n"
    "v_exp_f32 v163, v163\n"
    "v_cvt_pk_bf16_f32 v192, v160, v161\n"
    "v_exp_f32 v164, v164\n"
    "v_mfma_f32_32x32x16_bf16 v[112:127], v[208:211], v[196:199], v[112:127]\n"
    "s_waitcnt vmcnt(0)\n"
    "global_load_dwordx4 v[200:203], v224, s[40:41] offset:0 sc1\n"
    "global_load_dwordx4 v[204:207], v224, s[40:41] offset:1024 sc1\n"
    "global_load_dwordx4 v[208:211], v224, s[40:41] offset:2048 sc1\n"
    "s_add_u32 s40, s40, 3072\n"
    "s_addc_u32 s41, s41, 0\n"
    "v_exp_f32 v165, v165\n"
    "v_cvt_pk_bf16_f32 v193, v162, v163\n"
    "v_exp_f32 v166, v166\n"
    "v_exp_f32 v167, v167\n"
    "v_mfma_f32_32x32x16_bf16 v[176:191], v[212:215], v[132:135], 0\n"
    "v_cvt_pk_bf16_f32 v194, v164, v165\n"
    "v_cvt_pk_bf16_f32 v195, v166, v167\n"
    "v_exp_f32 v168, v168\n"
    "v_exp_f32 v169, v169\n"
    "v_mfma_f32_32x32x16_bf16 v[0:15], v[216:219], v[192:195], v[0:15]\n"
    "v_cvt_pk_bf16_f32 v196, v168, v169\n"
    "v_exp_f32 v170, v170\n"
    "v_exp_f32 v171, v171\n"
    "v_exp_f32 v172, v172\n"
    "v_cvt_pk_bf16_f32 v197, v170, v171\n"
    "v_exp_f32 v173, v173\n"
    "v_exp_f32 v174, v174\n"
    "v_cvt_pk_bf16_f32 v198, v172, v173\n"
    "v_exp_f32 v175, v175\n"
    "v_exp_f32 v176, v176\n"
    "v_exp_f32 v177, v177\n"
    "v_cvt_pk_bf16_f32 v199, v174, v175\n"
    "v_exp_f32 v178, v178\n"
    "v_exp_f32 v179, v179\n"
    "v_cvt_pk_bf16_f32 v192, v176, v177\n"
    "v_exp_f32 v180, v180\n"
    "v_mfma_f32_32x32x16_bf16 v[0:15], v[220:223], v[196:199], v[0:15]\n"
    "v_exp_f32 v181, v181\n"
    "v_cvt_pk_bf16_f32 v193, v178, v179\n"
    "v_exp_f32 v182, v182\n"
    "v_exp_f32 v183, v183\n"
    "v_mfma_f32_32x32x16_bf16 v[160:175], v[212:215], v[136:139], 0\n"
    "v_cvt_pk_bf16_f32 v194, v180, v181\n"
    "v_cvt_pk_bf16_f32 v195, v182, v183\n"
    "v_exp_f32 v184, v184\n"
    "v_exp_f32 v185, v185\n"
    "v_mfma_f32_32x32x16_bf16 v[16:31], v[216:219], v[192:195], v[16:31]\n"
    "v_cvt_pk_bf16_f32 v196, v184, v185\n"
    "v_exp_f32 v186, v186\n"
    "v_exp_f32 v187, v187\n"
    "v_exp_f32 v188, v188\n"
    "v_cvt_pk_bf16_f32 v197, v186, v187\n"
    "v_exp_f32 v189, v189\n"
    "v_exp_f32 v190, v190\n"
    "v_cvt_pk_bf16_f32 v198, v188, v189\n"
    "v_exp_f32 v191, v191\n"
    "v_exp_f32 v160, v160\n"
    "v_exp_f32 v161, v161\n"
    "v_cvt_pk_bf16_f32 v199, v190, v191\n"
    "v_exp_f32 v162, v162\n"
    "v_exp_f32 v163, v163\n"
    "v_cvt_pk_bf16_f32 v192, v160, v161\n"
    "v_exp_f32 v164, v164\n"
    "v_mfma_f32_32x32x16_bf16 v[16:31], v[220:223], v[196:199], v[16:31]\n"
    "v_exp_f32 v165, v165\n"
    "v_cvt_pk_bf16_f32 v193, v162, v163\n"
    "v_exp_f32 v166, v166\n"
    "v_exp_f32 v167, v167\n"
    "v_mfma_f32_32x32x16_bf16 v[176:191], v[212:215], v[140:143], 0\n"
    "v_cvt_pk_bf16_f32 v194, v164, v165\n"
    "v_cvt_pk_bf16_f32 v195, v166, v167\n"
    "v_exp_f32 v168, v168\n"
    "v_exp_f32 v169, v169\n"
    "v_mfma_f32_32x32x16_bf16 v[32:47], v[216:219], v[192:195], v[32:47]\n"
    "v_cvt_pk_bf16_f32 v196, v168, v169\n"
    "v_exp_f32 v170, v170\n"
    "v_exp_f32 v171, v171\n"
    "v_exp_f32 v172, v172\n"
    "v_cvt_pk_bf16_f32 v197, v170, v171\n"
    "v_exp_f32 v173, v173\n"
    "v_exp_f32 v174, v174\n"
    "v_cvt_pk_bf16_f32 v198, v172, v173\n"
    "v_exp_f32 v175, v175\n"
    "v_exp_f32 v176, v176\n"
    "v_exp_f32 v177, v177\n"
    "v_cvt_pk_bf16_f32 v199, v174, v175\n"
    "v_exp_f32 v178, v178\n"
    "v_exp_f32 v179, v179\n"
    "v_cvt_pk_bf16_f32 v192, v176, v177\n"
    "v_exp_f32 v180, v180\n"
    "v_mfma_f32_32x32x16_bf16 v[32:47], v[220:223], v[196:199], v[32:47]\n"
    "v_exp_f32 v181, v181\n"
    "v_cvt_pk_bf16_f32 v193, v178, v179\n"
    "v_exp_f32 v182, v182\n"
    "v_exp_f32 v183, v183\n"
    "v_mfma_f32_32x32x16_bf16 v[160:175], v[212:215], v[144:147], 0\n"
    "v_cvt_pk_bf16_f32 v194, v180, v181\n"
    "v_cvt_pk_bf16_f32 v195, v182, v183\n"
    "v_exp_f32 v184, v184\n"
    "v_exp_f32 v185, v185\n"
    "v_mfma_f32_32x32x16_bf16 v[48:63], v[216:219], v[192:195], v[48:63]\n"
    "v_cvt_pk_bf16_f32 v196, v184, v185\n"
    "v_exp_f32 v186, v186\n"
    "v_exp_f32 v187, v187\n"
    "v_exp_f32 v188, v188\n"
    "v_cvt_pk_bf16_f32 v197, v186, v187\n"
    "v_exp_f32 v189, v189\n"
    "v_exp_f32 v190, v190\n"
    "v_cvt_pk_bf16_f32 v198, v188, v189\n"
    "v_exp_f32 v191, v191\n"
    "v_exp_f32 v160, v160\n"
    "v_exp_f32 v161, v161\n"
    "v_cvt_pk_bf16_f32 v199, v190, v191\n"
    "v_exp_f32 v162, v162\n"
    "v_exp_f32 v163, v163\n"
    "v_cvt_pk_bf16_f32 v192, v160, v161\n"
    "v_exp_f32 v164, v164\n"
    "v_mfma_f32_32x32x16_bf16 v[48:63], v[220:223], v[196:199], v[48:63]\n"
    "v_exp_f32 v165, v165\n"
    "v_cvt_pk_bf16_f32 v193, v162, v163\n"
    "v_exp_f32 v166, v166\n"
    "v_exp_f32 v167, v167\n"
    "v_mfma_f32_32x32x16_bf16 v[176:191], v[212:215], v[148:151], 0\n"
    "v_cvt_pk_bf16_f32 v194, v164, v165\n"
    "v_cvt_pk_bf16_f32 v195, v166, v167\n"
    "v_exp_f32 v168, v168\n"
    "v_exp_f32 v169, v169\n"
    "v_mfma_f32_32x32x16_bf16 v[64:79], v[216:219], v[192:195], v[64:79]\n"
    "v_cvt_pk_bf16_f32 v196, v168, v169\n"
    "v_exp_f32 v170, v170\n"
    "v_exp_f32 v171, v171\n"
    "v_exp_f32 v172, v172\n"
    "v_cvt_pk_bf16_f32 v197, v170, v171\n"
    "v_exp_f32 v173, v173\n"
    "v_exp_f32 v174, v174\n"
    "v_cvt_pk_bf16_f32 v198, v172, v173\n"
    "v_exp_f32 v175, v175\n"
    "v_exp_f32 v176, v176\n"
    "v_exp_f32 v177, v177\n"
    "v_cvt_pk_bf16_f32 v199, v174, v175\n"
    "v_exp_f32 v178, v178\n"
    "v_exp_f32 v179, v179\n"
    "v_cvt_pk_bf16_f32 v192, v176, v177\n"
    "v_exp_f32 v180, v180\n"
    "v_mfma_f32_32x32x16_bf16 v[64:79], v[220:223], v[196:199], v[64:79]\n"
    "v_exp_f32 v181, v181\n"
    "v_cvt_pk_bf16_f32 v193, v178, v179\n"
    "v_exp_f32 v182, v182\n"
    "v_exp_f32 v183, v183\n"
    "v_mfma_f32_32x32x16_bf16 v[160:175], v[212:215], v[152:155], 0\n"
    "v_cvt_pk_bf16_f32 v194, v180, v181\n"
    "v_cvt_pk_bf16_f32 v195, v182, v183\n"
    "v_exp_f32 v184, v184\n"
    "v_exp_f32 v185, v185\n"
    "v_mfma_f32_32x32x16_bf16 v[80:95], v[216:219], v[192:195], v[80:95]\n"
    "v_cvt_pk_bf16_f32 v196, v184, v185\n"
    "v_exp_f32 v186, v186\n"
    "v_exp_f32 v187, v187\n"
    "v_exp_f32 v188, v188\n"
    "v_cvt_pk_bf16_f32 v197, v186, v187\n"
    "v_exp_f32 v189, v189\n"
    "v_exp_f32 v190, v190\n"
    "v_cvt_pk_bf16_f32 v198, v188, v189\n"
    "v_exp_f32 v191, v191\n"
    "v_exp_f32 v160, v160\n"
    "v_exp_f32 v161, v161\n"
    "v_cvt_pk_bf16_f32 v199, v190, v191\n"
    "v_exp_f32 v162, v162\n"
    "v_exp_f32 v163, v163\n"
    "v_cvt_pk_bf16_f32 v192, v160, v161\n"
    "v_exp_f32 v164, v164\n"
    "v_mfma_f32_32x32x16_bf16 v[80:95], v[220:223], v[196:199], v[80:95]\n"
    "v_exp_f32 v165, v165\n"
    "v_cvt_pk_bf16_f32 v193, v162, v163\n"
    "v_exp_f32 v166, v166\n"
    "v_exp_f32 v167, v167\n"
    "v_mfma_f32_32x32x16_bf16 v[176:191], v[212:215], v[156:159], 0\n"
    "v_cvt_pk_bf16_f32 v194, v164, v165\n"
    "v_cvt_pk_bf16_f32 v195, v166, v167\n"
    "v_exp_f32 v168, v168\n"
    "v_exp_f32 v169, v169\n"
    "v_mfma_f32_32x32x16_bf16 v[96:111], v[216:219], v[192:195], v[96:111]\n"
    "v_cvt_pk_bf16_f32 v196, v168, v169\n"
    "v_exp_f32 v170, v170\n"
    "v_exp_f32 v171, v171\n"
    "v_exp_f32 v172, v172\n"
    "v_cvt_pk_bf16_f32 v197, v170, v171\n"
    "v_exp_f32 v173, v173\n"
    "v_exp_f32 v174, v174\n"
    "v_cvt_pk_bf16_f32 v198, v172, v173\n"
    "v_exp_f32 v175, v175\n"
    "v_exp_f32 v176, v176\n"
    "v_exp_f32 v177, v177\n"
    "v_cvt_pk_bf16_f32 v199, v174, v175\n"
    "v_exp_f32 v178, v178\n"
    "v_exp_f32 v179, v179\n"
    "v_cvt_pk_bf16_f32 v192, v176, v177\n"
    "v_exp_f32 v180, v180\n"
    "v_mfma_f32_32x32x16_bf16 v[96:111], v[220:223], v[196:199], v[96:111]\n"
    "v_exp_f32 v181, v181\n"
    "v_cvt_pk_bf16_f32 v193, v178, v179\n"
    "v_exp_f32 v182, v182\n"
    "v_exp_f32 v183, v183\n"
    "s_waitcnt vmcnt(2)\n"
    "v_mfma_f32_32x32x16_bf16 v[160:175], v[200:203], v[128:131], 0\n"
    "v_cvt_pk_bf16_f32 v194, v180, v181\n"
    "v_cvt_pk_bf16_f32 v195, v182, v183\n"
    "v_exp_f32 v184, v184\n"
    "v_exp_f32 v185, v185\n"
    "v_mfma_f32_32x32x16_bf16 v[112:127], v[216:219], v[192:195], v[112:127]\n"
    "v_cvt_pk_bf16_f32 v196, v184, v185\n"
    "v_exp_f32 v186, v186\n"
    "v_exp_f32 v187, v187\n"
    "v_exp_f32 v188, v188\n"
    "v_cvt_pk_bf16_f32 v197, v186, v187\n"
    "v_exp_f32 v189, v189\n"
    "v_exp_f32 v190, v190\n"
    "v_cvt_pk_bf16_f32 v198, v188, v189\n"
    "v_exp_f32 v191, v191\n"
    "s_sub_u32 s42, s42, 1\n"
    "s_cmp_lg_u32 s42, 0\n"
    "s_cbranch_scc1 1b\n"
    "s_nop 0\n"
    "v_cvt_pk_bf16_f32 v199, v190, v191\n"
    "s_nop 1\n"
    "v_mfma_f32_32x32x16_bf16 v[112:127], v[220:223], v[196:199], v[112:127]\n"
    "s_waitcnt vmcnt(0)\n"
    "s_nop 7\n"
    "s_nop 7\n"
    : "={v[0:15]}"(o[0]), "={v[16:31]}"(o[1]), "={v[32:47]}"(o[2]), "={v[48:63]}"(o[3]), "={v[64:79]}"(o[4]), "={v[80:95]}"(o[5]), "={v[96:111]}"(o[6]), "={v[112:127]}"(o[7]), "+{s[40:41]}"(p), "+{s42}"(iters)
    : "{v[128:131]}"(q[0]), "{v[132:135]}"(q[1]), "{v[136:139]}"(q[2]), "{v[140:143]}"(q[3]), "{v[144:147]}"(q[4]), "{v[148:151]}"(q[5]), "{v[152:155]}"(q[6]), "{v[156:159]}"(q[7]), "{v224}"(voff)
    : "v160", "v161", "v162", "v163", "v164", "v165", "v166", "v167", "v168", "v169", "v170", "v171", "v172", "v173", "v174", "v175", "v176", "v177", "v178", "v179", "v180", "v181", "v182", "v183", "v184", "v185", "v186", "v187", "v188", "v189", "v190", "v191", "v192", "v193", "v194", "v195", "v196", "v197", "v198", "v199", "v200", "v201", "v202", "v203", "v204", "v205", "v206", "v207", "v208", "v209", "v210", "v211", "v212", "v213", "v214", "v215", "v216", "v217", "v218", "v219", "v220", "v221", "v222", "v223", "scc", "memory");
}


DI void kv_gemm(f32x16& a0, f32x16& a1, unsigned va, const unsigned (&xb)[8]) {
  asm volatile(
    "v_mov_b32 v0, 0\n"
    "v_mov_b32 v1, 0\n"
    "v_mov_b32 v2, 0\n"
    "v_mov_b32 v3, 0\n"
    "v_mov_b32 v4, 0\n"
    "v_mov_b32 v5, 0\n"
    "v_mov_b32 v6, 0\n"
    "v_mov_b32 v7, 0\n"
    "v_mov_b32 v8, 0\n"
    "v_mov_b32 v9, 0\n"
    "v_mov_b32 v10, 0\n"
    "v_mov_b32 v11, 0\n"
    "v_mov_b32 v12, 0\n"
    "v_mov_b32 v13, 0\n"
    "v_mov_b32 v14, 0\n"
    "v_mov_b32 v15, 0\n"
    "v_mov_b32 v16, 0\n"
    "v_mov_b32 v17, 0\n"
    "v_mov_b32 v18, 0\n"
    "v_mov_b32 v19, 0\n"
    "v_mov_b32 v20, 0\n"
    "v_mov_b32 v21, 0\n"
    "v_mov_b32 v22, 0\n"
    "v_mov_b32 v23, 0\n"
    "v_mov_b32 v24, 0\n"
    "v_mov_b32 v25, 0\n"
    "v_mov_b32 v26, 0\n"
    "v_mov_b32 v27, 0\n"
    "v_mov_b32 v28, 0\n"
    "v_mov_b32 v29, 0\n"
    "v_mov_b32 v30, 0\n"
    "v_mov_b32 v31, 0\n"
    "ds_read_b64_tr_b16 v[32:33], v72 offset:0\n"
    "ds_read_b64_tr_b16 v[34:35], v72 offset:128\n"
    "ds_read_b128 v[36:39], v74 offset:0\n"
    "ds_read_b64_tr_b16 v[40:41], v72 offset:512\n"
    "ds_read_b64_tr_b16 v[42:43], v72 offset:640\n"
    "ds_read_b128 v[44:47], v75 offset:0\n"
    "ds_read_b64_tr_b16 v[48:49], v72 offset:1024\n"
    "ds_read_b64_tr_b16 v[50:51], v72 offset:1152\n"
    "ds_read_b128 v[52:55], v76 offset:0\n"
    "ds_read_b64_tr_b16 v[56:57], v72 offset:1536\n"
    "ds_read_b64_tr_b16 v[58:59], v72 offset:1664\n"
    "ds_read_b128 v[60:63], v77 offset:0\n"
    "ds_read_b64_tr_b16 v[64:65], v72 offset:2048\n"
    "ds_read_b64_tr_b16 v[66:67], v72 offset:2176\n"
    "ds_read_b128 v[68:71], v78 offset:0\n"
    "s_waitcnt lgkmcnt(12)\n"
    "v_mfma_f32_32x32x16_bf16 v[0:15], v[32:35], v[36:39], v[0:15]\n"
    "ds_read_b64_tr_b16 v[32:33], v72 offset:2560\n"
    "ds_read_b64_tr_b16 v[34:35], v72 offset:2688\n"
    "ds_read_b128 v[36:39], v79 offset:0\n"
    "s_waitcnt lgkmcnt(12)\n"
    "v_mfma_f32_32x32x16_bf16 v[16:31], v[40:43], v[44:47], v[16:31]\n"
    "ds_read_b64_tr_b16 v[40:41], v72 offset:3072\n"
    "ds_read_b64_tr_b16 v[42:43], v72 offset:3200\n"
    "ds_read_b128 v[44:47], v80 offset:0\n"
    "s_waitcnt lgkmcnt(12)\n"
    "v_mfma_f32_32x32x16_bf16 v[0:15], v[48:51], v[52:55], v[0:15]\n"
    "ds_read_b64_tr_b16 v[48:49], v72 offset:3584\n"
    "ds_read_b64_tr_b16 v[50:51], v72 offset:3712\n"
    "ds_read_b128 v[52:55], v81 offset:0\n"
    "s_waitcnt lgkmcnt(12)\n"
    "v_mfma_f32_32x32x16_bf16 v[16:31], v[56:59], v[60:63], v[16:31]\n"
    "ds_read_b64_tr_b16 v[56:57], v72 offset:4096\n"
    "ds_read_b64_tr_b16 v[58:59], v72 offset:4224\n"
    "ds_read_b128 v[60:63], v74 offset:256\n"
    "s_waitcnt lgkmcnt(12)\n"
    "v_mfma_f32_32x32x16_bf16 v[0:15], v[64:67], v[68:71], v[0:15]\n"
    "ds_read_b64_tr_b16 v[64:65], v72 offset:4608\n"
    "ds_read_b64_tr_b16 v[66:67], v72 offset:4736\n"
    "ds_read_b128 v[68:71], v75 offset:256\n"
    "s_waitcnt lgkmcnt(12)\n"
    "v_mfma_f32_32x32x16_bf16 v[16:31], v[32:35], v[36:39], v[16:31]\n"
    "ds_read_b64_tr_b16 v[32:33], v72 offset:5120\n"
    "ds_read_b64_tr_b16 v[34:35], v72 offset:5248\n"
    "ds_read_b128 v[36:39], v76 offset:256\n"
    "s_waitcnt lgkmcnt(12)\n"
    "v_mfma_f32_32x32x16_bf16 v[0:15], v[40:43], v[44:47], v[0:15]\n"
    "ds_read_b64_tr_b16 v[40:41], v72 offset:5632\n"
    "ds_read_b64_tr_b16 v[42:43], v72 offset:5760\n"
    "ds_read_b128 v[44:47], v77 offset:256\n"
    "s_waitcnt lgkmcnt(12)\n"
    "v_mfma_f32_32x32x16_bf16 v[16:31], v[48:51], v[52:55], v[16:31]\n"
    "ds_read_b64_tr_b16 v[48:49], v72 offset:6144\n"
    "ds_read_b64_tr_b16 v[50:51], v72 offset:6272\n"
    "ds_read_b128 v[52:55], v78 offset:256\n"
    "s_waitcnt lgkmcnt(12)\n"
    "v_mfma_f32_32x32x16_bf16 v[0:15], v[56:59], v[60:63], v[0:15]\n"
    "ds_read_b64_tr_b16 v[56:57], v72 offset:6656\n"
    "ds_read_b64_tr_b16 v[58:59], v72 offset:6784\n"
    "ds_read_b128 v[60:63], v79 offset:256\n"
    "s_waitcnt lgkmcnt(12)\n"
    "v_mfma_f32_32x32x16_bf16 v[16:31], v[64:67], v[68:71], v[16:31]\n"
    "ds_read_b64_tr_b16 v[64:65], v72 offset:7168\n"
    "ds_read_b64_tr_b16 v[66:67], v72 offset:7296\n"
    "ds_read_b128 v[68:71], v80 offset:256\n"
    "s_waitcnt lgkmcnt(12)\n"
    "v_mfma_f32_32x32x16_bf16 v[0:15], v[32:35], v[36:39], v[0:15]\n"
    "ds_read_b64_tr_b16 v[32:33], v72 offset:7680\n"
    "ds_read_b64_tr_b16 v[34:35], v72 offset:7808\n"
    "ds_read_b128 v[36:39], v81 offset:256\n"
    "s_waitcnt lgkmcnt(12)\n"
    "v_mfma_f32_32x32x16_bf16 v[16:31], v[40:43], v[44:47], v[16:31]\n"
    "ds_read_b64_tr_b16 v[40:41], v72 offset:8192\n"
    "ds_read_b64_tr_b16 v[42:43], v72 offset:8320\n"
    "ds_read_b128 v[44:47], v74 offset:512\n"
    "s_waitcnt lgkmcnt(12)\n"
    "v_mfma_f32_32x32x16_bf16 v[0:15], v[48:51], v[52:55], v[0:15]\n"
    "ds_read_b64_tr_b16 v[48:49], v72 offset:8704\n"
    "ds_read_b64_tr_b16 v[50:51], v72 offset:8832\n"
    "ds_read_b128 v[52:55], v75 offset:512\n"
    "s_waitcnt lgkmcnt(12)\n"
    "v_mfma_f32_32x32x16_bf16 v[16:31], v[56:59], v[60:63], v[16:31]\n"
    "ds_read_b64_tr_b16 v[56:57], v72 offset:9216\n"
    "ds_read_b64_tr_b16 v[58:59], v72 offset:9344\n"
    "ds_read_b128 v[60:63], v76 offset:512\n"
    "s_waitcnt lgkmcnt(12)\n"
    "v_mfma_f32_32x32x16_bf16 v[0:15], v[64:67], v[68:71], v[0:15]\n"
    "ds_read_b64_tr_b16 v[64:65], v72 offset:9728\n"
    "ds_read_b64_tr_b16 v[66:67], v72 offset:9856\n"
    "ds_read_b128 v[68:71], v77 offset:512\n"
    "s_waitcnt lgkmcnt(12)\n"
    "v_mfma_f32_32x32x16_bf16 v[16:31], v[32:35], v[36:39], v[16:31]\n"
    "ds_read_b64_tr_b16 v[32:33], v72 offset:10240\n"
    "ds_read_b64_tr_b16 v[34:35], v72 offset:10368\n"
    "ds_read_b128 v[36:39], v78 offset:512\n"
    "s_waitcnt lgkmcnt(12)\n"
    "v_mfma_f32_32x32x16_bf16 v[0:15], v[40:43], v[44:47], v[0:15]\n"
    "ds_read_b64_tr_b16 v[40:41], v72 offset:10752\n"
    "ds_read_b64_tr_b16 v[42:43], v72 offset:10880\n"
    "ds_read_b128 v[44:47], v79 offset:512\n"
    "s_waitcnt lgkmcnt(12)\n"
    "v_mfma_f32_32x32x16_bf16 v[16:31], v[48:51], v[52:55], v[16:31]\n"
    "ds_read_b64_tr_b16 v[48:49], v72 offset:11264\n"
    "ds_read_b64_tr_b16 v[50:51], v72 offset:11392\n"
    "ds_read_b128 v[52:55], v80 offset:512\n"
    "s_waitcnt lgkmcnt(12)\n"
    "v_mfma_f32_32x32x16_bf16 v[0:15], v[56:59], v[60:63], v[0:15]\n"
    "ds_read_b64_tr_b16 v[56:57], v72 offset:11776\n"
    "ds_read_b64_tr_b16 v[58:59], v72 offset:11904\n"
    "ds_read_b128 v[60:63], v81 offset:512\n"
    "s_waitcnt lgkmcnt(12)\n"
    "v_mfma_f32_32x32x16_bf16 v[16:31], v[64:67], v[68:71], v[16:31]\n"
    "ds_read_b64_tr_b16 v[64:65], v72 offset:12288\n"
    "ds_read_b64_tr_b16 v[66:67], v72 offset:12416\n"
    "ds_read_b128 v[68:71], v74 offset:768\n"
    "s_waitcnt lgkmcnt(12)\n"
    "v_mfma_f32_32x32x16_bf16 v[0:15], v[32:35], v[36:39], v[0:15]\n"
    "ds_read_b64_tr_b16 v[32:33], v72 offset:12800\n"
    "ds_read_b64_tr_b16 v[34:35], v72 offset:12928\n"
    "ds_read_b128 v[36:39], v75 offset:768\n"
    "s_waitcnt lgkmcnt(12)\n"
    "v_mfma_f32_32x32x16_bf16 v[16:31], v[40:43], v[44:47], v[16:31]\n"
    "ds_read_b64_tr_b16 v[40:41], v72 offset:13312\n"
    "ds_read_b64_tr_b16 v[42:43], v72 offset:13440\n"
    "ds_read_b128 v[44:47], v76 offset:768\n"
    "s_waitcnt lgkmcnt(12)\n"
    "v_mfma_f32_32x32x16_bf16 v[0:15], v[48:51], v[52:55], v[0:15]\n"
    "ds_read_b64_tr_b16 v[48:49], v72 offset:13824\n"
    "ds_read_b64_tr_b16 v[50:51], v72 offset:13952\n"
    "ds_read_b128 v[52:55], v77 offset:768\n"
    "s_waitcnt lgkmcnt(12)\n"
    "v_mfma_f32_32x32x16_bf16 v[16:31], v[56:59], v[60:63], v[16:31]\n"
    "ds_read_b64_tr_b16 v[56:57], v72 offset:14336\n"
    "ds_read_b64_tr_b16 v[58:59], v72 offset:14464\n"
    "ds_read_b128 v[60:63], v78 offset:768\n"
    "s_waitcnt lgkmcnt(12)\n"
    "v_mfma_f32_32x32x16_bf16 v[0:15], v[64:67], v[68:71], v[0:15]\n"
    "ds_read_b64_tr_b16 v[64:65], v72 offset:14848\n"
    "ds_read_b64_tr_b16 v[66:67], v72 offset:14976\n"
    "ds_read_b128 v[68:71], v79 offset:768\n"
    "s_waitcnt lgkmcnt(12)\n"
    "v_mfma_f32_32x32x16_bf16 v[16:31], v[32:35], v[36:39], v[16:31]\n"
    "ds_read_b64_tr_b16 v[32:33], v72 offset:15360\n"
    "ds_read_b64_tr_b16 v[34:35], v72 offset:15488\n"
    "ds_read_b128 v[36:39], v80 offset:768\n"
    "s_waitcnt lgkmcnt(12)\n"
    "v_mfma_f32_32x32x16_bf16 v[0:15], v[40:43], v[44:47], v[0:15]\n"
    "ds_read_b64_tr_b16 v[40:41], v72 offset:15872\n"
    "ds_read_b64_tr_b16 v[42:43], v72 offset:16000\n"
    "ds_read_b128 v[44:47], v81 offset:768\n"
    "s_waitcnt lgkmcnt(12)\n"
    "v_mfma_f32_32x32x16_bf16 v[16:31], v[48:51], v[52:55], v[16:31]\n"
    "s_waitcnt lgkmcnt(9)\n"
    "v_mfma_f32_32x32x16_bf16 v[0:15], v[56:59], v[60:63], v[0:15]\n"
    "s_waitcnt lgkmcnt(6)\n"
    "v_mfma_f32_32x32x16_bf16 v[16:31], v[64:67], v[68:71], v[16:31]\n"
    "s_waitcnt lgkmcnt(3)\n"
    "v_mfma_f32_32x32x16_bf16 v[0:15], v[32:35], v[36:39], v[0:15]\n"
    "s_waitcnt lgkmcnt(0)\n"
    "v_mfma_f32_32x32x16_bf16 v[16:31], v[40:43], v[44:47], v[16:31]\n"
    "s_nop 7\n"
    "s_nop 7\n"
    : "={v[0:15]}"(a0), "={v[16:31]}"(a1)
    : "{v72}"(va), "{v74}"(xb[0]), "{v75}"(xb[1]), "{v76}"(xb[2]), "{v77}"(xb[3]), "{v78}"(xb[4]), "{v79}"(xb[5]), "{v80}"(xb[6]), "{v81}"(xb[7])
    : "v32", "v33", "v34", "v35", "v36", "v37", "v38", "v39", "v40", "v41", "v42", "v43", "v44", "v45", "v46", "v47", "v48", "v49", "v50", "v51", "v52", "v53", "v54", "v55", "v56", "v57", "v58", "v59", "v60", "v61", "v62", "v63", "v64", "v65", "v66", "v67", "v68", "v69", "v70", "v71", "memory");
}
DI void q_gemm(f32x16& a0, f32x16& a1, unsigned va, const unsigned (&xb)[8]) {
  asm volatile(
    "v_mov_b32 v0, 0\n"
    "v_mov_b32 v1, 0\n"
    "v_mov_b32 v2, 0\n"
    "v_mov_b32 v3, 0\n"
    "v_mov_b32 v4, 0\n"
    "v_mov_b32 v5, 0\n"
    "v_mov_b32 v6, 0\n"
    "v_mov_b32 v7, 0\n"
    "v_mov_b32 v8, 0\n"
    "v_mov_b32 v9, 0\n"
    "v_mov_b32 v10, 0\n"
    "v_mov_b32 v11, 0\n"
    "v_mov_b32 v12, 0\n"
    "v_mov_b32 v13, 0\n"
    "v_mov_b32 v14, 0\n"
    "v_mov_b32 v15, 0\n"
    "v_mov_b32 v16, 0\n"
    "v_mov_b32 v17, 0\n"
    "v_mov_b32 v18, 0\n"
    "v_mov_b32 v19, 0\n"
    "v_mov_b32 v20, 0\n"
    "v_mov_b32 v21, 0\n"
    "v_mov_b32 v22, 0\n"
    "v_mov_b32 v23, 0\n"
    "v_mov_b32 v24, 0\n"
    "v_mov_b32 v25, 0\n"
    "v_mov_b32 v26, 0\n"
    "v_mov_b32 v27, 0\n"
    "v_mov_b32 v28, 0\n"
    "v_mov_b32 v29, 0\n"
    "v_mov_b32 v30, 0\n"
    "v_mov_b32 v31, 0\n"
    "ds_read_b64_tr_b16 v[32:33], v80 offset:0\n"
    "ds_read_b64_tr_b16 v[34:35], v80 offset:512\n"
    "ds_read_b128 v[36:39], v82 offset:0\n"
    "ds_read_b128 v[40:43], v82 offset:32768\n"
    "ds_read_b64_tr_b16 v[44:45], v80 offset:2048\n"
    "ds_read_b64_tr_b16 v[46:47], v80 offset:2560\n"
    "ds_read_b128 v[48:51], v83 offset:0\n"
    "ds_read_b128 v[52:55], v83 offset:32768\n"
    "ds_read_b64_tr_b16 v[56:57], v80 offset:4096\n"
    "ds_read_b64_tr_b16 v[58:59], v80 offset:4608\n"
    "ds_read_b128 v[60:63], v84 offset:0\n"
    "ds_read_b128 v[64:67], v84 offset:32768\n"
    "s_waitcnt lgkmcnt(8)\n"
    "v_mfma_f32_32x32x16_bf16 v[0:15], v[32:35], v[36:39], v[0:15]\n"
    "v_mfma_f32_32x32x16_bf16 v[16:31], v[32:35], v[40:43], v[16:31]\n"
    "ds_read_b64_tr_b16 v[32:33], v80 offset:6144\n"
    "ds_read_b64_tr_b16 v[34:35], v80 offset:6656\n"
    "ds_read_b128 v[36:39], v85 offset:0\n"
    "ds_read_b128 v[40:43], v85 offset:32768\n"
    "s_waitcnt lgkmcnt(8)\n"
    "v_mfma_f32_32x32x16_bf16 v[0:15], v[44:47], v[48:51], v[0:15]\n"
    "v_mfma_f32_32x32x16_bf16 v[16:31], v[44:47], v[52:55], v[16:31]\n"
    "ds_read_b64_tr_b16 v[44:45], v80 offset:8192\n"
    "ds_read_b64_tr_b16 v[46:47], v80 offset:8704\n"
    "ds_read_b128 v[48:51], v86 offset:0\n"
    "ds_read_b128 v[52:55], v86 offset:32768\n"
    "s_waitcnt lgkmcnt(8)\n"
    "v_mfma_f32_32x32x16_bf16 v[0:15], v[56:59], v[60:63], v[0:15]\n"
    "v_mfma_f32_32x32x16_bf16 v[16:31], v[56:59], v[64:67], v[16:31]\n"
    "ds_read_b64_tr_b16 v[56:57], v80 offset:10240\n"
    "ds_read_b64_tr_b16 v[58:59], v80 offset:10752\n"
    "ds_read_b128 v[60:63], v87 offset:0\n"
    "ds_read_b128 v[64:67], v87 offset:32768\n"
    "s_waitcnt lgkmcnt(8)\n"
    "v_mfma_f32_32x32x16_bf16 v[0:15], v[32:35], v[36:39], v[0:15]\n"
    "v_mfma_f32_32x32x16_bf16 v[16:31], v[32:35], v[40:43], v[16:31]\n"
    "ds_read_b64_tr_b16 v[32:33], v80 offset:12288\n"
    "ds_read_b64_tr_b16 v[34:35], v80 offset:12800\n"
    "ds_read_b128 v[36:39], v88 offset:0\n"
    "ds_read_b128 v[40:43], v88 offset:32768\n"
    "s_waitcnt lgkmcnt(8)\n"
    "v_mfma_f32_32x32x16_bf16 v[0:15], v[44:47], v[48:51], v[0:15]\n"
    "v_mfma_f32_32x32x16_bf16 v[16:31], v[44:47], v[52:55], v[16:31]\n"
    "ds_read_b64_tr_b16 v[44:45], v80 offset:14336\n"
    "ds_read_b64_tr_b16 v[46:47], v80 offset:14848\n"
    "ds_read_b128 v[48:51], v89 offset:0\n"
    "ds_read_b128 v[52:55], v89 offset:32768\n"
    "s_waitcnt lgkmcnt(8)\n"
    "v_mfma_f32_32x32x16_bf16 v[0:15], v[56:59], v[60:63], v[0:15]\n"
    "v_mfma_f32_32x32x16_bf16 v[16:31], v[56:59], v[64:67], v[16:31]\n"
    "ds_read_b64_tr_b16 v[56:57], v80 offset:16384\n"
    "ds_read_b64_tr_b16 v[58:59], v80 offset:16896\n"
    "ds_read_b128 v[60:63], v82 offset:256\n"
    "ds_read_b128 v[64:67], v82 offset:33024\n"
    "s_waitcnt lgkmcnt(8)\n"
    "v_mfma_f32_32x32x16_bf16 v[0:15], v[32:35], v[36:39], v[0:15]\n"
    "v_mfma_f32_32x32x16_bf16 v[16:31], v[32:35], v[40:43], v[16:31]\n"
    "ds_read_b64_tr_b16 v[32:33], v80 offset:18432\n"
    "ds_read_b64_tr_b16 v[34:35], v80 offset:18944\n"
    "ds_read_b128 v[36:39], v83 offset:256\n"
    "ds_read_b128 v[40:43], v83 offset:33024\n"
    "s_waitcnt lgkmcnt(8)\n"
    "v_mfma_f32_32x32x16_bf16 v[0:15], v[44:47], v[48:51], v[0:15]\n"
    "v_mfma_f32_32x32x16_bf16 v[16:31], v[44:47], v[52:55], v[16:31]\n"
    "ds_read_b64_tr_b16 v[44:45], v80 offset:20480\n"
    "ds_read_b64_tr_b16 v[46:47], v80 offset:20992\n"
    "ds_read_b128 v[48:51], v84 offset:256\n"
    "ds_read_b128 v[52:55], v84 offset:33024\n"
    "s_waitcnt lgkmcnt(8)\n"
    "v_mfma_f32_32x32x16_bf16 v[0:15], v[56:59], v[60:63], v[0:15]\n"
    "v_mfma_f32_32x32x16_bf16 v[16:31], v[56:59], v[64:67], v[16:31]\n"
    "ds_read_b64_tr_b16 v[56:57], v80 offset:22528\n"
    "ds_read_b64_tr_b16 v[58:59], v80 offset:23040\n"
    "ds_read_b128 v[60:63], v85 offset:256\n"
    "ds_read_b128 v[64:67], v85 offset:33024\n"
    "s_waitcnt lgkmcnt(8)\n"
    "v_mfma_f32_32x32x16_bf16 v[0:15], v[32:35], v[36:39], v[0:15]\n"
    "v_mfma_f32_32x32x16_bf16 v[16:31], v[32:35], v[40:43], v[16:31]\n"
    "ds_read_b64_tr_b16 v[32:33], v80 offset:24576\n"
    "ds_read_b64_tr_b16 v[34:35], v80 offset:25088\n"
    "ds_read_b128 v[36:39], v86 offset:256\n"
    "ds_read_b128 v[40:43], v86 offset:33024\n"
    "s_waitcnt lgkmcnt(8)\n"
    "v_mfma_f32_32x32x16_bf16 v[0:15], v[44:47], v[48:51], v[0:15]\n"
    "v_mfma_f32_32x32x16_bf16 v[16:31], v[44:47], v[52:55], v[16:31]\n"
    "ds_read_b64_tr_b16 v[44:45], v80 offset:26624\n"
    "ds_read_b64_tr_b16 v[46:47], v80 offset:27136\n"
    "ds_read_b128 v[48:51], v87 offset:256\n"
    "ds_read_b128 v[52:55], v87 offset:33024\n"
    "s_waitcnt lgkmcnt(8)\n"
    "v_mfma_f32_32x32x16_bf16 v[0:15], v[56:59], v[60:63], v[0:15]\n"
    "v_mfma_f32_32x32x16_bf16 v[16:31], v[56:59], v[64:67], v[16:31]\n"
    "ds_read_b64_tr_b16 v[56:57], v80 offset:28672\n"
    "ds_read_b64_tr_b16 v[58:59], v80 offset:29184\n"
    "ds_read_b128 v[60:63], v88 offset:256\n"
    "ds_read_b128 v[64:67], v88 offset:33024\n"
    "s_waitcnt lgkmcnt(8)\n"
    "v_mfma_f32_32x32x16_bf16 v[0:15], v[32:35], v[36:39], v[0:15]\n"
    "v_mfma_f32_32x32x16_bf16 v[16:31], v[32:35], v[40:43], v[16:31]\n"
    "ds_read_b64_tr_b16 v[32:33], v80 offset:30720\n"
    "ds_read_b64_tr_b16 v[34:35], v80 offset:31232\n"
    "ds_read_b128 v[36:39], v89 offset:256\n"
    "ds_read_b128 v[40:43], v89 offset:33024\n"
    "s_waitcnt lgkmcnt(8)\n"
    "v_mfma_f32_32x32x16_bf16 v[0:15], v[44:47], v[48:51], v[0:15]\n"
    "v_mfma_f32_32x32x16_bf16 v[16:31], v[44:47], v[52:55], v[16:31]\n"
    "s_waitcnt lgkmcnt(4)\n"
    "v_mfma_f32_32x32x16_bf16 v[0:15], v[56:59], v[60:63], v[0:15]\n"
    "v_mfma_f32_32x32x16_bf16 v[16:31], v[56:59], v[64:67], v[16:31]\n"
    "s_waitcnt lgkmcnt(0)\n"
    "v_mfma_f32_32x32x16_bf16 v[0:15], v[32:35], v[36:39], v[0:15]\n"
    "v_mfma_f32_32x32x16_bf16 v[16:31], v[32:35], v[40:43], v[16:31]\n"
    "s_nop 7\n"
    "s_nop 7\n"
    : "={v[0:15]}"(a0), "={v[16:31]}"(a1)
    : "{v80}"(va), "{v82}"(xb[0]), "{v83}"(xb[1]), "{v84}"(xb[2]), "{v85}"(xb[3]), "{v86}"(xb[4]), "{v87}"(xb[5]), "{v88}"(xb[6]), "{v89}"(xb[7])
    : "v32", "v33", "v34", "v35", "v36", "v37", "v38", "v39", "v40", "v41", "v42", "v43", "v44", "v45", "v46", "v47", "v48", "v49", "v50", "v51", "v52", "v53", "v54", "v55", "v56", "v57", "v58", "v59", "v60", "v61", "v62", "v63", "v64", "v65", "v66", "v67", "memory");
}
DI void wo_gemm(f32x16& a0, f32x16& a1, unsigned va0, unsigned va1, const u32x4 (&bf)[8]) {
  asm volatile(
    "v_mov_b32 v0, 0\n"
    "v_mov_b32 v1, 0\n"
    "v_mov_b32 v2, 0\n"
    "v_mov_b32 v3, 0\n"
    "v_mov_b32 v4, 0\n"
    "v_mov_b32 v5, 0\n"
    "v_mov_b32 v6, 0\n"
    "v_mov_b32 v7, 0\n"
    "v_mov_b32 v8, 0\n"
    "v_mov_b32 v9, 0\n"
    "v_mov_b32 v10, 0\n"
    "v_mov_b32 v11, 0\n"
    "v_mov_b32 v12, 0\n"
    "v_mov_b32 v13, 0\n"
    "v_mov_b32 v14, 0\n"
    "v_mov_b32 v15, 0\n"
    "v_mov_b32 v16, 0\n"
    "v_mov_b32 v17, 0\n"
    "v_mov_b32 v18, 0\n"
    "v_mov_b32 v19, 0\n"
    "v_mov_b32 v20, 0\n"
    "v_mov_b32 v21, 0\n"
    "v_mov_b32 v22, 0\n"
    "v_mov_b32 v23, 0\n"
    "v_mov_b32 v24, 0\n"
    "v_mov_b32 v25, 0\n"
    "v_mov_b32 v26, 0\n"
    "v_mov_b32 v27, 0\n"
    "v_mov_b32 v28, 0\n"
    "v_mov_b32 v29, 0\n"
    "v_mov_b32 v30, 0\n"
    "v_mov_b32 v31, 0\n"
    "s_nop 1\n"
    "ds_read_b64_tr_b16 v[32:33], v64 offset:0\n"
    "ds_read_b64_tr_b16 v[34:35], v64 offset:1024\n"
    "ds_read_b64_tr_b16 v[36:37], v65 offset:0\n"
    "ds_read_b64_tr_b16 v[38:39], v65 offset:1024\n"
    "ds_read_b64_tr_b16 v[40:41], v64 offset:2048\n"
    "ds_read_b64_tr_b16 v[42:43], v64 offset:3072\n"
    "ds_read_b64_tr_b16 v[44:45], v65 offset:2048\n"
    "ds_read_b64_tr_b16 v[46:47], v65 offset:3072\n"
    "ds_read_b64_tr_b16 v[48:49], v64 offset:4096\n"
    "ds_read_b64_tr_b16 v[50:51], v64 offset:5120\n"
    "ds_read_b64_tr_b16 v[52:53], v65 offset:4096\n"
    "ds_read_b64_tr_b16 v[54:55], v65 offset:5120\n"
    "s_waitcnt lgkmcnt(8)\n"
    "v_mfma_f32_32x32x16_bf16 v[0:15], v[32:35], v[68:71], v[0:15]\n"
    "v_mfma_f32_32x32x16_bf16 v[16:31], v[36:39], v[68:71], v[16:31]\n"
    "ds_read_b64_tr_b16 v[32:33], v64 offset:6144\n"
    "ds_read_b64_tr_b16 v[34:35], v64 offset:7168\n"
    "ds_read_b64_tr_b16 v[36:37], v65 offset:6144\n"
    "ds_read_b64_tr_b16 v[38:39], v65 offset:7168\n"
    "s_waitcnt lgkmcnt(8)\n"
    "v_mfma_f32_32x32x16_bf16 v[0:15], v[40:43], v[72:75], v[0:15]\n"
    "v_mfma_f32_32x32x16_bf16 v[16:31], v[44:47], v[72:75], v[16:31]\n"
    "ds_read_b64_tr_b16 v[40:41], v64 offset:8192\n"
    "ds_read_b64_tr_b16 v[42:43], v64 offset:9216\n"
    "ds_read_b64_tr_b16 v[44:45], v65 offset:8192\n"
    "ds_read_b64_tr_b16 v[46:47], v65 offset:9216\n"
    "s_waitcnt lgkmcnt(8)\n"
    "v_mfma_f32_32x32x16_bf16 v[0:15], v[48:51], v[76:79], v[0:15]\n"
    "v_mfma_f32_32x32x16_bf16 v[16:31], v[52:55], v[76:79], v[16:31]\n"
    "ds_read_b64_tr_b16 v[48:49], v64 offset:10240\n"
    "ds_read_b64_tr_b16 v[50:51], v64 offset:11264\n"
    "ds_read_b64_tr_b16 v[52:53], v65 offset:10240\n"
    "ds_read_b64_tr_b16 v[54:55], v65 offset:11264\n"
    "s_waitcnt lgkmcnt(8)\n"
    "v_mfma_f32_32x32x16_bf16 v[0:15], v[32:35], v[80:83], v[0:15]\n"
    "v_mfma_f32_32x32x16_bf16 v[16:31], v[36:39], v[80:83], v[16:31]\n"
    "ds_read_b64_tr_b16 v[32:33], v64 offset:12288\n"
    "ds_read_b64_tr_b16 v[34:35], v64 offset:13312\n"
    "ds_read_b64_tr_b16 v[36:37], v65 offset:12288\n"
    "ds_read_b64_tr_b16 v[38:39], v65 offset:13312\n"
    "s_waitcnt lgkmcnt(8)\n"
    "v_mfma_f32_32x32x16_bf16 v[0:15], v[40:43], v[84:87], v[0:15]\n"
    "v_mfma_f32_32x32x16_bf16 v[16:31], v[44:47], v[84:87], v[16:31]\n"
    "ds_read_b64_tr_b16 v[40:41], v64 offset:14336\n"
    "ds_read_b64_tr_b16 v[42:43], v64 offset:15360\n"
    "ds_read_b64_tr_b16 v[44:45], v65 offset:14336\n"
    "ds_read_b64_tr_b16 v[46:47], v65 offset:15360\n"
    "s_waitcnt lgkmcnt(8)\n"
    "v_mfma_f32_32x32x16_bf16 v[0:15], v[48:51], v[88:91], v[0:15]\n"
    "v_mfma_f32_32x32x16_bf16 v[16:31], v[52:55], v[88:91], v[16:31]\n"
    "s_waitcnt lgkmcnt(4)\n"
    "v_mfma_f32_32x32x16_bf16 v[0:15], v[32:35], v[92:95], v[0:15]\n"
    "v_mfma_f32_32x32x16_bf16 v[16:31], v[36:39], v[92:95], v[16:31]\n"
    "s_waitcnt lgkmcnt(0)\n"
    "v_mfma_f32_32x32x16_bf16 v[0:15], v[40:43], v[96:99], v[0:15]\n"
    "v_mfma_f32_32x32x16_bf16 v[16:31], v[44:47], v[96:99], v[16:31]\n"
    "s_nop 7\n"
    "s_nop 7\n"
    : "={v[0:15]}"(a0), "={v[16:31]}"(a1)
    : "{v64}"(va0), "{v65}"(va1), "{v[68:71]}"(bf[0]), "{v[72:75]}"(bf[1]), "{v[76:79]}"(bf[2]), "{v[80:83]}"(bf[3]), "{v[84:87]}"(bf[4]), "{v[88:91]}"(bf[5]), "{v[92:95]}"(bf[6]), "{v[96:99]}"(bf[7])
    : "v32", "v33", "v34", "v35", "v36", "v37", "v38", "v39", "v40", "v41", "v42", "v43", "v44", "v45", "v46", "v47", "v48", "v49", "v50", "v51", "v52", "v53", "v54", "v55", "memory");
}


DI f32x2 unpk(unsigned u) { return f32x2{bflo(u), bfhi(u)}; }
DI void lf_set(int i, unsigned t, int lane) { WAITL(); if (lane == 0) __hip_atomic_store((LAS unsigned*)(size_t)(L_F + 4 * i), t, __ATOMIC_RELAXED, __HIP_MEMORY_SCOPE_WORKGROUP); }
template <int SLEEP = 1> DI void lf_wait(int i, int n, unsigned t, int lane) {
  const unsigned a = L_F + 4 * (i + (lane < n ? lane : 0));
  while (!__all(__hip_atomic_load((LAS unsigned*)(size_t)a, __ATOMIC_RELAXED, __HIP_MEMORY_SCOPE_WORKGROUP) == t)) __builtin_amdgcn_s_sleep(SLEEP);
  asm volatile("" ::: "memory");
}
DI void put_tile(unsigned a, const f32x16& t, int lane) {
#pragma unroll
  for (int g = 0; g < 4; g++) ldsw<f32x4>(a + 1024 * g + 16 * lane, f32x4{t[4 * g], t[4 * g + 1], t[4 * g + 2], t[4 * g + 3]});
}
DI void add_tile(unsigned a, f32x16& t, int lane) {
#pragma unroll
  for (int g = 0; g < 4; g++) { const f32x4 v = ldsr<f32x4>(a + 1024 * g + 16 * lane); t[4 * g] += v[0]; t[4 * g + 1] += v[1]; t[4 * g + 2] += v[2]; t[4 * g + 3] += v[3]; }
}
DI void pack16(const f32x16& a, float sc, u32x4& lo, u32x4& hi) {
  float t[16];
#pragma unroll
  for (int i = 0; i < 16; i++) t[i] = sc * a[i];
  lo = pack8u(t); hi = pack8u(t + 8);
}

__global__ void __launch_bounds__(512, 2) fused_kernel(const u16* __restrict__ x, const u16* __restrict__ wq, const u16* __restrict__ wk, const u16* __restrict__ wv,
                                                       const u16* __restrict__ wo, const u16* __restrict__ wg, const u16* __restrict__ wu, const u16* __restrict__ wd,
                                                       char* __restrict__ ws, u16* __restrict__ out) {
  extern __shared__ __attribute__((aligned(16))) char smem[];
  const int lane = threadIdx.x & 63, w = __builtin_amdgcn_readfirstlane(threadIdx.x >> 6);
  const int xcd = blockIdx.x & 7, slot = blockIdx.x >> 3;
  const int pp = (xcd & 1) * 16 + (slot & 15), j = (xcd >> 1) * 2 + (slot >> 4);
  const int r = lane & 31, h = lane >> 5, tq = (lane & 15) >> 2, tp = lane & 3, tg = lane >> 4;
  u64* flags = (u64*)(ws + WS_FLAG);
  u64 epoch;
  asm volatile("global_load_dwordx2 %0, %1, %2 sc1" : "=v"(epoch) : "v"(0u), "s"(flags + FL_EPOCH) : "memory");
  const __amdgpu_buffer_rsrc_t rs = __builtin_amdgcn_make_buffer_rsrc(ws, 0, (int)WS_END, 0x00020000);
  const bool kvcu = j < 2;
  const int tsw = ((tq >> 1) & 1) << 2;
  const unsigned wrow = lane >> 3, wch = 16 * ((lane & 7) ^ (((wrow >> 1) & 1) << 2));
  const unsigned tc0 = 8 * (tp & 1) + 16 * ((2 * (tg & 1) + (tp >> 1)) ^ tsw), tc1 = 8 * (tp & 1) + 16 * ((4 + 2 * (tg & 1) + (tp >> 1)) ^ tsw);
  u32x4 warm = {};

  {
    const u16* xb = x + (size_t)(pp * 64 + 8 * w) * DM;
#pragma unroll
    for (int i = 0; i < 8; i++) glds(16 * (lane ^ ((8 * w + i) & 15)), xb + i * DM, L_X + 1024 * (8 * w + i));
    __builtin_amdgcn_s_barrier();
#pragma unroll
    for (int i = 0; i < 4; i++) {
      const int s = 4 * w + i;
      glds(wrow * DM * 2 + wch, wq + (size_t)16 * s * DM + 64 * j, L_W + 2048 * s);
      glds(wrow * DM * 2 + wch, wq + (size_t)(16 * s + 8) * DM + 64 * j, L_W + 2048 * s + 1024);
    }
    if (kvcu) {
#pragma unroll
      for (int i = 0; i < 2; i++) {
        glds(16 * lane, wv + 512 * (2 * w + i), L_U + 1024 * (2 * w + i));
        glds(16 * lane, wk + 512 * (2 * w + i), L_U + 16384 + 1024 * (2 * w + i));
      }
    }
    if (w >= 4) {
      const int sh = pp & 15;
      const unsigned vo = (lane >> 5) * FF * 2 + 16 * (lane & 31);
#pragma unroll
      for (int i = 0; i < 8; i++) {
        const int t = 8 * (w - 4) + i;
        const char* src = (const char*)(t < 16 ? wg : wu) + (size_t)(32 * sh + 2 * (t & 15)) * FF * 2 + ((448 * j) & ~127);
        asm volatile("global_load_dwordx4 %0, %1, %2" : "+{v[252:255]}"(warm) : "v"(vo), "s"(src) : "memory");
      }
    } else {
      u64 pc; int len;
      asm volatile("s_getpc_b64 %0\n.Lfused_code_from:\n\ts_mov_b32 %1, .Lfused_code_to-.Lfused_code_from" : "=s"(pc), "=s"(len));
      const int kb = slot + 32 * w;
      const u64 src = uni64(pc + (1024 * (kb + 1) <= len ? 1024 * (u64)kb : 0));
#pragma unroll
      for (int i = 0; i < 8; i++) asm volatile("global_load_dwordx4 %0, %1, %2" : "+{v[252:255]}"(warm) : "v"(i == 0 ? lane * 16 : 0), "s"(src) : "memory");
    }
    if (kvcu) WAITV(20); else WAITV(16);
    if (!kvcu && threadIdx.x < 32) ldsw<unsigned>(L_F + 4 * threadIdx.x, 0u);
    const int row = 8 * w + (lane >> 3), part = lane & 7;
    u32x4 xf[8];
#pragma unroll
    for (int i = 0; i < 8; i++) xf[i] = ldsr<u32x4>(L_X + 1024 * row + 16 * ((part + 8 * i) ^ (row & 15)));
    float s1 = 0.f, s2 = 0.f, s1b = 0.f, s2b = 0.f;
#pragma unroll
    for (int i = 0; i < 8; i += 2) {
      const bf16x8 b0 = __builtin_bit_cast(bf16x8, xf[i]), b1 = __builtin_bit_cast(bf16x8, xf[i + 1]);
      s1 = sum8(b0, s1); s2 = dot8(b0, b0, s2); s1b = sum8(b1, s1b); s2b = dot8(b1, b1, s2b);
    }
    s1 += s1b; s2 += s2b;
    s1 = sum8lanes(s1); s2 = sum8lanes(s2);
    const float mean = s1 * (1.0f / DM), rstd = rsqrtf(fmaxf(s2 * (1.0f / DM) - mean * mean, 0.f) + 1e-5f);
    const f32x2 r2 = {rstd, rstd}, o2 = {-mean * rstd, -mean * rstd};
#pragma unroll
    for (int i = 0; i < 8; i++) {
      u32x4 y;
#pragma unroll
      for (int e = 0; e < 4; e++) { const f32x2 t = __builtin_elementwise_fma(unpk(xf[i][e]), r2, o2); y[e] = cvtpk(t[0], t[1]); }
      ldsw<u32x4>(L_X + 1024 * row + 16 * ((part + 8 * i) ^ (row & 15)), y);
    }
    WAITV(8);
  }
  asm volatile("" : "+v"(epoch));
  const u64 tag = uni64((epoch + 1) & 0xffffffffffffull);
  BAR();
  unsigned xb[8];
#pragma unroll
  for (int b = 0; b < 8; b++) xb[b] = L_X + 1024 * r + 16 * ((2 * b + h) ^ (r & 15));

  const unsigned t32 = (unsigned)tag, lt = t32 | 1u;
  const int g = w - 4;
  f32x16 qa0, qa1, kva;
  if (w < 4) {
    const int kh = w >> 1, nb = w & 1;
    unsigned xq[8];
#pragma unroll
    for (int b = 0; b < 8; b++) xq[b] = xb[b] + 512 * kh;
    q_gemm(qa0, qa1, L_W + 32768 * kh + 128 * (8 * h + tq) + (nb ? tc1 : tc0), xq);
  } else if (w == 7 && kvcu) {
    unsigned xq[8];
#pragma unroll
    for (int b = 0; b < 8; b++) xq[b] = xb[b] + 32768 * j;
    f32x16 k1;
    kv_gemm(kva, k1, L_U + 16384 * (tg & 1) + 32 * (8 * h + tq) + 8 * tp, xq);
    if (lane < 32) ldsw<unsigned>(L_F + 4 * lane, 0u);
#pragma unroll
    for (int e = 0; e < 16; e++) kva[e] += k1[e];
  }
  BAR();
  if (!(w == 7 && kvcu)) WAITV(0);
  const int cb = 224 * j, cpair = cb + 32 * (j & 1), cmix = cb + ((j & 1) ? 0 : 192), c0 = g < 3 ? cpair + 64 * g : cmix;
  const int rot = 8 * (pp & 3);
  const unsigned wstep = 16 * FF * 2, ring = L_W + 16384 * (g & 3);
  u64 w0 = uni64((u64)(wg + c0) + (u64)rot * wstep), w1 = uni64((u64)(wu + c0) + (u64)rot * wstep);
  const unsigned vw0 = g < 3 ? wrow * FF * 2 + wch : ((lane >> 2) * FF + 8 * (lane & 3)) * 2, vw1 = vw0 + 8 * FF * 2;
  if (w < 4) {
    const unsigned qp = w < 3 ? L_U + 4096 + 8192 * w : L_U + 24576, qpp = (w ^ 2) < 3 ? L_U + 4096 + 8192 * (w ^ 2) : L_U + 24576;
    put_tile(qp, (w >> 1) ? qa0 : qa1, lane);
    lf_set(F_QP + w, lt, lane);
    if (w == 3 && lane == 0) st_word(flags, FL_O + 16 * pp + j, tag << 16);
    lf_wait(F_QP + (w ^ 2), 1, lt, lane);
    f32x16 q = (w >> 1) ? qa1 : qa0;
    add_tile(qpp, q, lane);
    lf_set(F_RD + w, lt, lane);
    u32x4 lo, hi; pack16(q, QSCALE, lo, hi);
    lf_wait(F_RD, 4, lt, lane);
    const int nb = w & 1, p = w >> 1;
    ldsw<u32x4>(L_Q + 1024 * (4 * nb + p) + 16 * lane, lo); ldsw<u32x4>(L_Q + 1024 * (4 * nb + 2 + p) + 16 * lane, hi);
    lf_set(F_QF + w, lt, lane);
    lf_wait(F_QF, 4, lt, lane);
  } else if (w < 7) {
    p1g4_prefill(w0, w1, vw0, vw1, wstep, ring);
  } else {
    if (kvcu) {
      float val[16];
#pragma unroll
      for (int e = 0; e < 16; e++) val[e] = kva[e];
      const int ks = 2 * pp + j;
      const u32x4 kf = pack8u(val + 8);
      st16_wt(rs, WS_KV + ks * 3072 + lane * 16, kf);
      ldsw<u32x2>(L_VT + 32 * r + 8 * h, u32x2{cvtpk(val[0], val[1]), cvtpk(val[2], val[3])});
      ldsw<u32x2>(L_VT + 32 * r + 16 + 8 * h, u32x2{cvtpk(val[4], val[5]), cvtpk(val[6], val[7])});
      WAITL();
      const unsigned one = (r == 16 || r == 20) ? 0x3f803f80u : 0u;
      const unsigned a = L_VT + 32 * (4 * (tg >> 1) + tq) + 8 * tp;
      u32x4 vf0 = __builtin_bit_cast(u32x4, trfrag(a, a + 256)), vf1 = __builtin_bit_cast(u32x4, trfrag(a + 512, a + 768));
      if (r >= 16) { vf0 = u32x4{one, one, one, one}; vf1 = vf0; }
      st16_wt(rs, WS_KV + ks * 3072 + 1024 + lane * 16, vf0); st16_wt(rs, WS_KV + ks * 3072 + 2048 + lane * 16, vf1);
      float kn = dot8(__builtin_bit_cast(bf16x8, kf), __builtin_bit_cast(bf16x8, kf), 0.f); kn += xhalf(kn);
      const bool bad = __any(kn != kn);
      kn = max64(kn);
      const unsigned kvword = bad ? 0x7fc0u : ((__float_as_uint(kn) + 0xffffu) >> 16);
      WAITV(0);
      if (lane < KV_REP) st_word(flags, FL_KV + 64 * lane + 2 * pp + j, (tag << 16) | kvword);
    }
    p1g2_prefill(w0, w1, vw0, vw0, wstep, ring);
  }
  asm volatile("" :: "{v[252:255]}"(warm));

  f32x16 o[8];
  bool fast = true;
  const unsigned vap0 = 128 * (4 * h + tq) + tc0, vap1 = 128 * (4 * h + tq) + tc1;
  const unsigned vwd = wrow * DM * 2 + wch;
  if (w < 4) {
    bf16x8 q[8];
#pragma unroll
    for (int i = 0; i < 8; i++) q[i] = ldsr<bf16x8>(L_Q + 1024 * i + 16 * lane);
    float qn = 0.f; bool qbad = false;
#pragma unroll
    for (int i = 0; i < 8; i++) { float t = dot8(q[i], q[i], 0.f); t += xhalf(t); qbad |= (t != t); qn = fmaxf(qn, t); }
    qn = __any(qbad) ? __uint_as_float(0x7fc00000u) : max64(qn);
    float kn;
    if (w == 0) {
      kn = __uint_as_float((unsigned)(wait_words(flags, FL_KV + 64 * (blockIdx.x & (KV_REP - 1)) + lane, true, tag) & 0xffffu) << 16);
      kn = __any(kn != kn) ? __uint_as_float(0x7fc00000u) : max64(kn);
      if (lane == 0) ldsw<float>(L_F + 4 * F_KN, kn);
      lf_set(F_KV, lt, lane);
    } else {
      u32x4 dm[6];
      const int sh = pp & 15;
#pragma unroll
      for (int i = 0; i < 6; i++) {
        const int t = 6 * (w - 1) + i;
        const u16* src = t < 14 ? wd + (size_t)(112 * sh + 8 * t) * DM : wo + (size_t)(32 * sh + 8 * (t - 14)) * DM;
        asm volatile("global_load_dwordx4 %0, %1, %2" : "=v"(dm[i]) : "v"((lane >> 3) * DM * 2 + 16 * (lane & 7)), "s"(src + 64 * j) : "memory");
      }
      lf_wait(F_KV, 1, lt, lane);
      WAITV(0);
      asm volatile("" :: "v"(dm[0]), "v"(dm[1]), "v"(dm[2]), "v"(dm[3]), "v"(dm[4]), "v"(dm[5]));
      kn = ldsr<float>(L_F + 4 * F_KN);
    }
    fast = __builtin_amdgcn_readfirstlane((int)(qn * kn <= FAST_BOUND * FAST_BOUND)) != 0;
    if (w == 0 && lane == 0) ldsw<unsigned>(L_F + 4 * F_FAST, fast ? lt : 2u);
    if (fast) attn_loop(o, q, ws + WS_KV + (size_t)w * 16 * 3072, lane * 16, 16 / ATTN_U);
  } else {
    __builtin_amdgcn_s_setprio(2);
    const unsigned wrap = 3 - (pp & 3);
#pragma unroll
    for (int b = 0; b < 8; b++) xb[b] += 32 * rot;
    if (g < 3) {
      const unsigned va[4] = {ring + 128 * (8 * h + tq) + tc0, ring + 128 * (8 * h + tq) + tc0 + 512, ring + 128 * (8 * h + tq) + tc1, ring + 128 * (8 * h + tq) + tc1 + 512};
      f32x16 acc[8];
      p1g4_loop(acc, uni64(w0), uni64(w1), vw0, vw1, va, xb, wstep, ring, wrap);
#pragma unroll
      for (int nb = 0; nb < 2; nb++)
#pragma unroll
        for (int p = 0; p < 2; p++) {
          u32x4 lo, hi; geglu16(acc[2 * nb + p], acc[4 + 2 * nb + p], lo, hi);
          const unsigned fo = WS_HF + ((2 * pp + p) * 112 + c0 / 16 + 2 * nb) * 1024 + lane * 16;
          st16_wt(rs, fo, lo); st16_wt(rs, fo + 1024, hi);
        }
    } else {
      const unsigned a0 = ring + 64 * (8 * h + tq) + 32 * (tg & 1) + 8 * tp;
      const unsigned va[4] = {a0, a0 + 256, a0, a0 + 256};
      f32x16 acc[4];
      p1g2_loop(acc, uni64(w0), uni64(w1), vw0, vw0, va, xb, wstep, ring, wrap);
#pragma unroll
      for (int p = 0; p < 2; p++) {
        u32x4 lo, hi; geglu16(acc[p], acc[2 + p], lo, hi);
        const unsigned fo = WS_HF + ((2 * pp + p) * 112 + c0 / 16) * 1024 + lane * 16;
        st16_wt(rs, fo, lo); st16_wt(rs, fo + 1024, hi);
      }
    }
#pragma unroll
    for (int i = 0; i < 8; i++) {
      const int s = 8 * g + i;
      glds(vwd, wo + (size_t)16 * s * DM + 64 * j, L_W + 2048 * s);
      glds(vwd, wo + (size_t)(16 * s + 8) * DM + 64 * j, L_W + 2048 * s + 1024);
    }
    WAITV(16);
    lf_set(F_P1 + g, lt, lane);
    if (blockIdx.x == 0 && g == 3) {
      for (unsigned spins = 0; spins < (1u << 16); ++spins) {
        bool ok = true;
#pragma unroll
        for (int c = 0; c < 4; c++) ok &= (ld_word(flags, FL_O + 16 * (8 * c + (lane >> 3)) + (lane & 7)) >> 16) == tag;
        if (__all(ok)) break;
        __builtin_amdgcn_s_sleep(8);
      }
      if (lane == 0) st_word(flags, FL_EPOCH, tag);
    }
    const unsigned ring2 = L_X + 16384 * g;
    const int rot2 = 4 * (pp % 7);
    u64 wdp = uni64((u64)(wd + (size_t)(448 * g + 16 * rot2) * DM + 64 * j));
    unsigned wrapw = (28 - rot2) / 4;
    lf_wait<8>(F_P1, 4, lt, lane);
    if (g == 0 && lane == 0) st_word(flags, FL_H + 16 * pp + j, tag << 16);
    p2_prefill(wdp, vwd, vwd + 8 * DM * 2, ring2, wrapw);
    wait_words<4>(flags, FL_H + 16 * pp + 2 * g + (lane & 1), lane < 2, tag);
    f32x16 d00, d01, d10, d11;
    p2_run(d00, d01, d10, d11, wdp, (u64)(ws + WS_HF + ((size_t)(2 * pp) * 112 + 28 * g + rot2) * 1024),
           (u64)(ws + WS_HF + ((size_t)(2 * pp + 1) * 112 + 28 * g + rot2) * 1024), ring2 + vap0, ring2 + vap1, vwd, vwd + 8 * DM * 2, lane * 16, ring2, wrapw, (28 - rot2) / 4);
    if (g != 0) put_tile(ring2, d00, lane);
    if (g != 1) put_tile(ring2 + 4096 * (1 > g ? 0 : 1), d01, lane);
    if (g != 2) put_tile(ring2 + 4096 * (2 > g ? 1 : 2), d10, lane);
    if (g != 3) put_tile(ring2 + 8192, d11, lane);
    lf_set(F_DP + g, lt, lane);
    lf_wait<2>(F_DP, 4, lt, lane);
    f32x16 d = g == 0 ? d00 : g == 1 ? d01 : g == 2 ? d10 : d11;
#pragma unroll
    for (int u = 0; u < 4; u++) if (u != g) add_tile(L_X + 16384 * u + 4096 * (g > u ? g - 1 : g), d, lane);
    float y[16];
#pragma unroll
    for (int e = 0; e < 16; e++) y[e] = d[e] * __builtin_amdgcn_rcpf(1.0f + __builtin_amdgcn_exp2f(-1.4426950408889634f * d[e]));
    {
      const int grow = 32 * (g & 1) + r;
#pragma unroll
      for (int gq = 0; gq < 4; gq++)
        ldsw<u32x2>(L_G + 128 * grow + 16 * ((4 * (g >> 1) + gq) ^ (grow & 7)) + 8 * h, u32x2{cvtpk(y[4 * gq], y[4 * gq + 1]), cvtpk(y[4 * gq + 2], y[4 * gq + 3])});
    }
    WAITV(0);
    __builtin_amdgcn_s_setprio(0);
  }
  BAR();
  if (w < 4) {
    if (fast) {
#pragma unroll
      for (int i = 0; i < 8; i++) {
        const unsigned p = opart(w * 8 + i);
        ldsw<f32x4>(p + 16 * lane, f32x4{o[i][0], o[i][1], o[i][2], o[i][3]});
        ldsw<f32x4>(p + 1024 + 16 * lane, f32x4{o[i][4], o[i][5], o[i][6], o[i][7]});
        ldsw<float>(p + 2048 + 4 * r + 128 * h, h ? 0.f : o[i][8]);
      }
    } else {
#pragma unroll 1
      for (int i = 0; i < 8; i++) {
        const bf16x8 q = ldsr<bf16x8>(L_Q + 1024 * i + 16 * lane);
        f32x16 oa = {}; float m = -3.0e38f;
#pragma unroll 1
        for (int ks = 0; ks < 16; ks++) {
          const unsigned ko = (unsigned)(WS_KV + (w * 16 + ks) * 3072 + lane * 16);
          const bf16x8 kf = __builtin_bit_cast(bf16x8, ld16_wt(rs, ko)), v0 = __builtin_bit_cast(bf16x8, ld16_wt(rs, ko + 1024)), v1 = __builtin_bit_cast(bf16x8, ld16_wt(rs, ko + 2048));
          const f32x16 s = mfma(kf, q, f32x16{});
          float mx = s[0];
#pragma unroll
          for (int e = 1; e < 16; e++) mx = fmaxf(mx, s[e]);
          mx = fmaxf(mx, xhalf(mx));
          const float mn = fmaxf(m, mx), al = __builtin_amdgcn_exp2f(m - mn);
          m = mn;
          float p[16];
#pragma unroll
          for (int e = 0; e < 16; e++) { p[e] = __builtin_amdgcn_exp2f(s[e] - mn); oa[e] *= al; }
          oa = mfma(v0, pack8(p), oa); oa = mfma(v1, pack8(p + 8), oa);
        }
        const unsigned p = opart(w * 8 + i);
        ldsw<f32x4>(p + 16 * lane, f32x4{oa[0], oa[1], oa[2], oa[3]});
        ldsw<f32x4>(p + 1024 + 16 * lane, f32x4{oa[4], oa[5], oa[6], oa[7]});
        ldsw<float>(p + 2048 + 4 * r + 128 * h, h ? m : oa[8]);
      }
    }
  }
  BAR();
  {
    const int i = w;
    float v[9] = {};
    if ((unsigned)__builtin_amdgcn_readfirstlane(ldsr<unsigned>(L_F + 4 * F_FAST)) == lt) {
      f32x4 a[4], b[4]; float l[4];
#pragma unroll
      for (int u = 0; u < 4; u++) { const unsigned p = opart(u * 8 + i); a[u] = ldsr<f32x4>(p + 16 * lane); b[u] = ldsr<f32x4>(p + 1024 + 16 * lane); l[u] = ldsr<float>(p + 2048 + 4 * r); }
#pragma unroll
      for (int e = 0; e < 4; e++) { v[e] = (a[0][e] + a[1][e]) + (a[2][e] + a[3][e]); v[4 + e] = (b[0][e] + b[1][e]) + (b[2][e] + b[3][e]); }
      v[8] = (l[0] + l[1]) + (l[2] + l[3]);
    } else {
      float m[4], mx = -3.0e38f;
#pragma unroll
      for (int u = 0; u < 4; u++) { m[u] = ldsr<float>(opart(u * 8 + i) + 2176 + 4 * r); mx = fmaxf(mx, m[u]); }
#pragma unroll
      for (int u = 0; u < 4; u++) {
        const float sc = __builtin_amdgcn_exp2f(m[u] - mx);
        const unsigned p = opart(u * 8 + i);
        const f32x4 a = ldsr<f32x4>(p + 16 * lane), b = ldsr<f32x4>(p + 1024 + 16 * lane); const float l = ldsr<float>(p + 2048 + 4 * r);
#pragma unroll
        for (int e = 0; e < 4; e++) { v[e] += sc * a[e]; v[4 + e] += sc * b[e]; }
        v[8] += sc * l;
      }
    }
    const float inv = 1.0f / v[8];
#pragma unroll
    for (int e = 0; e < 8; e++) v[e] *= inv;
    const u32x4 ov = pack8u(v);
    const unsigned oo = WS_OF + ((2 * pp + (i & 1)) * 32 + 4 * j + (i >> 1)) * 2048 + lane * 16;
    st16_wt(rs, oo, u32x4{t32, ov[0], t32, ov[1]}); st16_wt(rs, oo + 1024, u32x4{t32, ov[2], t32, ov[3]});
  }
  BAR();
  {
    const int p = w & 1, kq = w >> 1;
    const int frow = 8 * w + (lane >> 3), fc = lane & 7;
    const size_t ooff = (size_t)(pp * 64 + frow) * DM + 64 * j + 8 * fc;
    const u32x4 xa = *(const u32x4*)(x + ooff);
    bf16x8 f0[8], f1[8];
#pragma unroll
    for (int u = 0; u < 8; u++) { const unsigned a = L_W + 2048 * (4 * u + kq); f0[u] = trfrag(a + vap0, a + vap0 + 1024); f1[u] = trfrag(a + vap1, a + vap1 + 1024); }
    const u32x4 gp = ldsr<u32x4>(L_G + 128 * frow + 16 * (fc ^ (frow & 7)));
    const unsigned ob = WS_OF + ((2 * pp + p) * 32 + kq) * 2048;
    f32x16 s0 = {}, s1 = {};
    auto round = [&](auto first, auto count) {
      constexpr int F0 = decltype(first)::value, N = decltype(count)::value;
      u32x4 bf[N];
      for (unsigned spins = 0;; ++spins) {
        const u32x2 pr = __builtin_amdgcn_raw_buffer_load_b64(rs, ob + 8192 * (F0 + (lane % N)) + 1024 + 1016, 0, 16);
        if (__all(pr[0] == t32)) {
          u32x4 g0[N], g1[N];
#pragma unroll
          for (int u = 0; u < N; u++) { g0[u] = ld16_wt(rs, ob + 8192 * (F0 + u) + lane * 16); g1[u] = ld16_wt(rs, ob + 8192 * (F0 + u) + 1024 + lane * 16); }
          bool good = true;
#pragma unroll
          for (int u = 0; u < N; u++) { good &= (g0[u][0] == t32) & (g0[u][2] == t32) & (g1[u][0] == t32) & (g1[u][2] == t32); bf[u] = u32x4{g0[u][1], g0[u][3], g1[u][1], g1[u][3]}; }
          if (__all(good)) break;
        }
        __builtin_amdgcn_s_sleep(1);
        if ((spins & 63u) == 63u) {
          if (__any((ld_word(flags, FL_TMO) >> 16) == tag)) break;
          if (spins > (1u << 16)) { st_word(flags, FL_TMO, tag << 16); break; }
        }
      }
#pragma unroll
      for (int u = 0; u < N; u++) { s0 = mfma(f0[F0 + u], __builtin_bit_cast(bf16x8, bf[u]), s0); s1 = mfma(f1[F0 + u], __builtin_bit_cast(bf16x8, bf[u]), s1); }
    };
    round(std::integral_constant<int, 2>{}, std::integral_constant<int, 6>{});
    round(std::integral_constant<int, 0>{}, std::integral_constant<int, 2>{});
#pragma unroll
    for (int gq = 0; gq < 4; gq++) {
      ldsw<f32x4>(L_X + (w * 2 + 0) * 4096 + 1024 * gq + 16 * ((lane & 48) | ((lane + 2 * gq) & 15)), f32x4{s0[4 * gq], s0[4 * gq + 1], s0[4 * gq + 2], s0[4 * gq + 3]});
      ldsw<f32x4>(L_X + (w * 2 + 1) * 4096 + 1024 * gq + 16 * ((lane & 48) | ((lane + 8 + 2 * gq) & 15)), f32x4{s1[4 * gq], s1[4 * gq + 1], s1[4 * gq + 2], s1[4 * gq + 3]});
    }
    BAR();
    f32x4 sa = {}, sb = {};
    {
      const int fr = frow & 31, fp = frow >> 5;
      const unsigned a0 = L_X + (fp * 2 + (fc >> 2)) * 4096 + 1024 * (fc & 3) + 16 * ((fr & 16) | ((fr + 2 * fc) & 15));
#pragma unroll
      for (int u = 0; u < 4; u++) { sa += ldsr<f32x4>(a0 + 16384 * u); sb += ldsr<f32x4>(a0 + 16384 * u + 512); }
    }
    const u32x4 yo = {cvtpk(bflo(xa[0]) + sa[0] * bflo(gp[0]), bfhi(xa[0]) + sa[1] * bfhi(gp[0])), cvtpk(bflo(xa[1]) + sa[2] * bflo(gp[1]), bfhi(xa[1]) + sa[3] * bfhi(gp[1])),
                      cvtpk(bflo(xa[2]) + sb[0] * bflo(gp[2]), bfhi(xa[2]) + sb[1] * bfhi(gp[2])), cvtpk(bflo(xa[3]) + sb[2] * bflo(gp[3]), bfhi(xa[3]) + sb[3] * bfhi(gp[3]))};
    *(u32x4*)(out + ooff) = yo;
  }
  asm volatile(".Lfused_code_to:");
}

extern "C" void kernel_launch(void* const* d_in, const int* in_sizes, int n_in, void* d_out, int out_size, void* d_ws, size_t ws_size, hipStream_t stream) {
  const u16* x = (const u16*)d_in[0]; const u16* wq = (const u16*)d_in[1]; const u16* wk = (const u16*)d_in[2]; const u16* wv = (const u16*)d_in[3]; const u16* wo = (const u16*)d_in[4];
  const u16* wg = (const u16*)d_in[6]; const u16* wu = (const u16*)d_in[7]; const u16* wd = (const u16*)d_in[8];
  char* ws = (char*)d_ws;
  (void)hipFuncSetAttribute((const void*)fused_kernel, hipFuncAttributeMaxDynamicSharedMemorySize, LDS_BYTES);
  fused_kernel<<<256, 512, LDS_BYTES, stream>>>(x, wq, wk, wv, wo, wg, wu, wd, ws, (u16*)d_out);
}
```
